```python
import torch

def _softmax(x, axis=-1):
    return torch.softmax(x, dim=axis)
def _gelu(x):
    return torch.nn.functional.gelu(x, approximate="tanh")
def _silu(x):
    return torch.nn.functional.silu(x)
def _sigmoid(x):
    return torch.sigmoid(x)
def _layernorm(x, axis=-1, eps=1e-5):
    m = x.mean(dim=axis, keepdim=True)
    v = x.var(dim=axis, keepdim=True, unbiased=False)
    return (x - m) / torch.sqrt(v + eps)
def _scatter_add(tgt, idx, src, axis=0):
    return tgt.clone().index_add_(axis, idx, src)
def _segment_sum(src, seg, n):
    out = torch.zeros((n,) + src.shape[1:], dtype=src.dtype)
    return out.index_add_(0, seg, src)
def _pad(x, pw):
    flat = tuple(v for pair in reversed(pw) for v in pair)
    return torch.nn.functional.pad(x, flat)

kernel_name = 'sequential_preLN__sliding_block_cached__dense_activation_gelu__decode'

def setup_inputs():

    _w = (lambda a: (lambda t: t.to(torch.bfloat16) if t.is_floating_point() else t)(torch.as_tensor(a)))
    def _A_setup():
        B, Sq, D = 1, 1, 512
        Hq, Hkv, Sk, Dh = 32, 32, 16384, 128
        assert Hq % Hkv == 0, (Hq, Hkv)
        return {
            "x": _w((lambda *s: torch.randn(*s, dtype=torch.bfloat16) * 0.1)(B, Sq, D)),
            "k_cache": _w((lambda *s: torch.randn(*s, dtype=torch.bfloat16) * 1.6)(B, Hkv, Sk, Dh)),
            "v_cache": _w((lambda *s: torch.randn(*s, dtype=torch.bfloat16) * 1.6)(B, Hkv, Sk, Dh)),
            "wq": _w((lambda *s: torch.randn(*s, dtype=torch.bfloat16) * 1.6)(D, Hq * Dh) * (D ** -0.5)),
            "wo": _w((lambda *s: torch.randn(*s, dtype=torch.bfloat16) * 1.6)(Hq * Dh, D) * ((Hq * Dh) ** -0.5)),
        }
    _A_in = _A_setup()

    def _B_setup():
        B, S, D, F = 1, 1, 512, 1376
        return {
            "x": _w((lambda *s: torch.randn(*s, dtype=torch.bfloat16) * 0.1)(B, S, D)),
            "w1": _w((lambda *s: torch.randn(*s, dtype=torch.bfloat16) * 3.2)(D, F) * (D ** -0.5)),
            "b1": _w((lambda *s: torch.randn(*s, dtype=torch.bfloat16) * 3.2)(F) * 0.01),
            "w2": _w((lambda *s: torch.randn(*s, dtype=torch.bfloat16) * 3.2)(F, D) * (F ** -0.5)),
            "b2": _w((lambda *s: torch.randn(*s, dtype=torch.bfloat16) * 3.2)(D) * 0.01),
        }
    _B_in = _B_setup()

    return {
        "A_x": _A_in["x"],
        "A_k_cache": _A_in["k_cache"],
        "A_v_cache": _A_in["v_cache"],
        "A_wq": _A_in["wq"],
        "A_wo": _A_in["wo"],
        "B_x": _B_in["x"],
        "B_w1": _B_in["w1"],
        "B_b1": _B_in["b1"],
        "B_w2": _B_in["w2"],
        "B_b2": _B_in["b2"],
    }

def reference(A_x, A_k_cache, A_v_cache, A_wq, A_wo, B_x, B_w1, B_b1, B_w2, B_b2):
    def _A_ref(x, k_cache, v_cache, wq, wo):
        B, Sq, D = x.shape
        Hq, Dh = 32, 128
        Hkv, Sk = k_cache.shape[1], k_cache.shape[2]
        W = min(4096, max(256, Sk // 4))
        rep = Hq // Hkv
        q = (lambda x, *d: x.permute(*d))((x @ wq).reshape(B, Sq, Hq, Dh), 0, 2, 1, 3)
        k = (lambda x, n, axis: torch.repeat_interleave(x, n, dim=axis))(k_cache[:, :, Sk - W:, :], rep, axis=1)
        v = (lambda x, n, axis: torch.repeat_interleave(x, n, dim=axis))(v_cache[:, :, Sk - W:, :], rep, axis=1)
        scores = torch.einsum("bhqd,bhkd->bhqk", q, k) * (Dh ** -0.5)
        p = _softmax(scores, axis=-1)
        o = torch.einsum("bhqk,bhkd->bhqd", p, v)
        return (lambda x, *d: x.permute(*d))(o, 0, 2, 1, 3).reshape(B, Sq, Hq * Dh) @ wo

    def _B_ref(x, w1, b1, w2, b2):
        h = torch.einsum("bsd,df->bsf", x, w1) + b1
        h = _gelu(h)
        return torch.einsum("bsf,fd->bsd", h, w2) + b2

    x = A_x
    h = x + _A_ref(_layernorm(x, axis=-1), A_k_cache, A_v_cache, A_wq, A_wo)
    h2 = h + _B_ref(_layernorm(h, axis=-1), B_w1, B_b1, B_w2, B_b2)
    return h2
```

```cpp
#include <hip/hip_runtime.h>
#include <hip/hip_bf16.h>
#include <stdint.h>

typedef unsigned v4u __attribute__((ext_vector_type(4)));
typedef float f32x4 __attribute__((ext_vector_type(4)));
typedef __bf16 bf16x2 __attribute__((ext_vector_type(2)));
typedef unsigned short u16;

constexpr int D = 512, H = 32, SK = 16384, DH = 128, W = 4096, F = 1376;

__device__ __forceinline__ float bf2f(unsigned b) { return __builtin_bit_cast(float, b << 16); }
__device__ __forceinline__ unsigned f2bf(float f) { unsigned u = __builtin_bit_cast(unsigned, f); return (u + 0x7fffu + ((u >> 16) & 1u)) >> 16; }
__device__ __forceinline__ float rbf(float f) { return bf2f(f2bf(f)); }
__device__ __forceinline__ float dot2(unsigned k, unsigned q, float acc) {
  return __builtin_amdgcn_fdot2_f32_bf16(__builtin_bit_cast(bf16x2, k), __builtin_bit_cast(bf16x2, q), acc, false);
}
__device__ __forceinline__ float wave_sum(float v) { for (int o = 32; o > 0; o >>= 1) v += __shfl_xor(v, o); return v; }
__device__ __forceinline__ float wave_max(float v) { for (int o = 32; o > 0; o >>= 1) v = fmaxf(v, __shfl_xor(v, o)); return v; }

__device__ void ln512(const float* in, float* out, int tid) {
  int lane = tid & 63;
  float v[8]; float s = 0.f;
  for (int j = 0; j < 8; ++j) { v[j] = in[lane * 8 + j]; s += v[j]; }
  s = wave_sum(s);
  float mean = s * (1.f / 512.f);
  float q = 0.f;
  for (int j = 0; j < 8; ++j) { float d = v[j] - mean; q += d * d; }
  q = wave_sum(q);
  float var = rbf(q * (1.f / 512.f));
  float mb = rbf(mean);
  float den = rbf(sqrtf(rbf(var + 1e-5f)));
  if (tid < 64) for (int j = 0; j < 8; ++j) out[lane * 8 + j] = rbf(rbf(v[j] - mb) / den);
}

__global__ void k_q(const u16* x, const u16* wq, u16* q) {
  __shared__ float xs[512], xn[512], red[4][64];
  int tid = threadIdx.x;
  xs[tid] = bf2f(x[tid]); xs[tid + 256] = bf2f(x[tid + 256]);
  __syncthreads();
  ln512(xs, xn, tid);
  __syncthreads();
  int col = blockIdx.x * 64 + (tid & 63), kq = tid >> 6;
  float acc = 0.f;
  for (int i = kq * 128; i < kq * 128 + 128; ++i) acc += xn[i] * bf2f(wq[(size_t)i * 4096 + col]);
  red[kq][tid & 63] = acc;
  __syncthreads();
  if (tid < 64) q[col] = f2bf(red[0][tid] + red[1][tid] + red[2][tid] + red[3][tid]);
}

__global__ void __launch_bounds__(512) k_attn(const u16* q, const u16* kc, const u16* vc, float* part) {
  __shared__ float sm[8][132];
  int tid = threadIdx.x, lane = tid & 63, w = tid >> 6, r = lane >> 4, c = lane & 15;
  int h = blockIdx.x >> 3, s = blockIdx.x & 7;
  size_t base = ((size_t)h * SK + (SK - W) + s * 512 + w * 64) * DH;
  const v4u* kp = (const v4u*)(kc + base) + lane;
  const v4u* vp = (const v4u*)(vc + base) + lane;
  v4u K[16], V[16];
  for (int i = 0; i < 16; ++i) K[i] = __builtin_nontemporal_load(kp + i * 64);
  for (int i = 0; i < 16; ++i) V[i] = __builtin_nontemporal_load(vp + i * 64);
  v4u qq = *(const v4u*)(q + h * DH + c * 8);
  float p[16]; float m = -INFINITY;
  for (int i = 0; i < 16; ++i) {
    float d = 0.f;
    d = dot2(K[i].x, qq.x, d); d = dot2(K[i].y, qq.y, d); d = dot2(K[i].z, qq.z, d); d = dot2(K[i].w, qq.w, d);
    d += __shfl_xor(d, 1); d += __shfl_xor(d, 2); d += __shfl_xor(d, 4); d += __shfl_xor(d, 8);
    d = rbf(rbf(d) * 0.08838834764831845f);
    p[i] = d; m = fmaxf(m, d);
  }
  m = fmaxf(m, __shfl_xor(m, 16)); m = fmaxf(m, __shfl_xor(m, 32));
  float l = 0.f; float acc[8] = {0, 0, 0, 0, 0, 0, 0, 0};
  for (int i = 0; i < 16; ++i) {
    float e = __expf(p[i] - m); l += e; e = rbf(e);
    acc[0] += e * bf2f(V[i].x & 0xffff); acc[1] += e * bf2f(V[i].x >> 16);
    acc[2] += e * bf2f(V[i].y & 0xffff); acc[3] += e * bf2f(V[i].y >> 16);
    acc[4] += e * bf2f(V[i].z & 0xffff); acc[5] += e * bf2f(V[i].z >> 16);
    acc[6] += e * bf2f(V[i].w & 0xffff); acc[7] += e * bf2f(V[i].w >> 16);
  }
  l += __shfl_xor(l, 16); l += __shfl_xor(l, 32);
  for (int j = 0; j < 8; ++j) { acc[j] += __shfl_xor(acc[j], 16); acc[j] += __shfl_xor(acc[j], 32); }
  if (r == 0) for (int j = 0; j < 8; ++j) sm[w][c * 8 + j] = acc[j];
  if (lane == 0) { sm[w][128] = m; sm[w][129] = l; }
  __syncthreads();
  if (tid < 128) {
    float M = -INFINITY; for (int u = 0; u < 8; ++u) M = fmaxf(M, sm[u][128]);
    float L = 0.f, o = 0.f;
    for (int u = 0; u < 8; ++u) { float wt = __expf(sm[u][128] - M); L += wt * sm[u][129]; o += wt * sm[u][tid]; }
    float* pp = part + (size_t)blockIdx.x * 132;
    pp[tid] = o; if (tid == 0) { pp[128] = M; pp[129] = L; }
  }
}

__global__ void k_wo(const float* part, const u16* wo, float* y) {
  __shared__ float o[128]; __shared__ float red[2][128];
  int tid = threadIdx.x, h = blockIdx.x >> 2, cb = blockIdx.x & 3;
  if (tid < 128) {
    float M = -INFINITY; for (int s = 0; s < 8; ++s) M = fmaxf(M, part[(h * 8 + s) * 132 + 128]);
    float L = 0.f, a = 0.f;
    for (int s = 0; s < 8; ++s) { const float* pp = part + (h * 8 + s) * 132; float wt = __expf(pp[128] - M); L += wt * pp[129]; a += wt * pp[tid]; }
    o[tid] = rbf(a / L);
  }
  __syncthreads();
  int col = cb * 128 + (tid & 127), half = tid >> 7;
  float acc = 0.f;
  for (int d = half * 64; d < half * 64 + 64; ++d) acc += o[d] * bf2f(wo[(size_t)(h * 128 + d) * 512 + col]);
  red[half][tid & 127] = acc;
  __syncthreads();
  if (tid < 128) y[h * 512 + col] = red[0][tid] + red[1][tid];
}

__global__ void k_mlp1(const u16* x, const float* y, const u16* w1, const u16* b1, float* hout, float* h1) {
  __shared__ float hs[512], hn[512], red[8][32];
  int tid = threadIdx.x;
  for (int j = tid; j < 512; j += 256) {
    float a = 0.f; for (int h = 0; h < 32; ++h) a += y[h * 512 + j];
    hs[j] = rbf(bf2f(x[j]) + rbf(a));
    if (blockIdx.x == 0) hout[j] = hs[j];
  }
  __syncthreads();
  ln512(hs, hn, tid);
  __syncthreads();
  int col = blockIdx.x * 32 + (tid & 31), kq = tid >> 5;
  float acc = 0.f;
  for (int i = kq * 64; i < kq * 64 + 64; ++i) acc += hn[i] * bf2f(w1[(size_t)i * F + col]);
  red[kq][tid & 31] = acc;
  __syncthreads();
  if (tid < 32) {
    float a = 0.f; for (int u = 0; u < 8; ++u) a += red[u][tid];
    a = rbf(rbf(a) + bf2f(b1[col]));
    float t = 0.7978845608028654f * (a + 0.044715f * a * a * a);
    float g = 0.5f * a * (1.f + tanhf(t));
    h1[col] = rbf(g);
  }
}

__global__ void k_mlp2(const float* hin, const float* h1, const u16* w2, const u16* b2, u16* out) {
  __shared__ float hs[F], red[8][32];
  int tid = threadIdx.x;
  for (int j = tid; j < F; j += 256) hs[j] = h1[j];
  __syncthreads();
  int col = blockIdx.x * 32 + (tid & 31), kq = tid >> 5;
  float acc = 0.f;
  for (int i = kq * 172; i < kq * 172 + 172; ++i) acc += hs[i] * bf2f(w2[(size_t)i * 512 + col]);
  red[kq][tid & 31] = acc;
  __syncthreads();
  if (tid < 32) {
    float a = 0.f; for (int u = 0; u < 8; ++u) a += red[u][tid];
    a = rbf(rbf(a) + bf2f(b2[col]));
    out[col] = f2bf(hin[col] + a);
  }
}

extern "C" void kernel_launch(void* const* d_in, const int* in_sizes, int n_in, void* d_out, int out_size, void* d_ws, size_t ws_size, hipStream_t stream) {
  const u16* x = (const u16*)d_in[0]; const u16* kc = (const u16*)d_in[1]; const u16* vc = (const u16*)d_in[2];
  const u16* wq = (const u16*)d_in[3]; const u16* wo = (const u16*)d_in[4];
  const u16* w1 = (const u16*)d_in[6]; const u16* b1 = (const u16*)d_in[7]; const u16* w2 = (const u16*)d_in[8]; const u16* b2 = (const u16*)d_in[9];
  char* ws = (char*)d_ws;
  u16* q = (u16*)ws; float* part = (float*)(ws + 16384); float* y = (float*)(ws + 262144); float* hh = (float*)(ws + 524288); float* h1 = (float*)(ws + 524288 + 4096);
  k_q<<<64, 256, 0, stream>>>(x, wq, q);
  k_attn<<<256, 512, 0, stream>>>(q, kc, vc, part);
  k_wo<<<128, 256, 0, stream>>>(part, wo, y);
  k_mlp1<<<43, 256, 0, stream>>>(x, y, w1, b1, hh, h1);
  k_mlp2<<<16, 256, 0, stream>>>(hh, h1, w2, b2, (u16*)d_out);
}
```

```cpp
#include <hip/hip_runtime.h>
#include <stdint.h>

typedef unsigned v4u __attribute__((ext_vector_type(4)));
typedef unsigned v2u __attribute__((ext_vector_type(2)));
typedef float f32x4 __attribute__((ext_vector_type(4)));
typedef float f32x2 __attribute__((ext_vector_type(2)));
typedef short v4s __attribute__((ext_vector_type(4)));
typedef __bf16 bf16x2 __attribute__((ext_vector_type(2)));
typedef __bf16 bf16x8 __attribute__((ext_vector_type(8)));
typedef unsigned short u16;
#define LAS __attribute__((address_space(3)))
#define GAS __attribute__((address_space(1)))

namespace {
constexpr int SK = 16384, DH = 128, WIN = 4096, FF = 1376;
constexpr int NWG = 256, NTHR = 576;
constexpr int WS_EPOCH = 0;
constexpr int WS_QG = 4096;
constexpr int WS_XM = WS_QG + 16384;
constexpr int WS_XO = 32768;
constexpr int WS_GG = WS_XO + 262144;
constexpr int WS_H1 = WS_GG + 32768;
constexpr int WS_END = WS_H1 + 8192;
constexpr int WS_STAMP = 1 << 20;
constexpr int L_W = 0;
constexpr int L_Q = 32768;
constexpr int L_FLAG = L_Q + 256;
constexpr int L_ML = L_FLAG + 64;
constexpr int L_O = L_ML + 64;
constexpr int L_SCR = L_O + 4096;
constexpr int L_RED = L_SCR + 4096;
constexpr int L_YP = L_RED + 1024;
constexpr int L_H = L_YP + 4096;
constexpr int L_END = L_H + 64;
constexpr int LDS_BYTES = 84 * 1024;
static_assert(L_END <= LDS_BYTES, "lds");
constexpr int AUX_NT = 2, AUX_SC1 = 16;
constexpr unsigned SPIN_CAP = 1u << 16;
constexpr float LOG2E = 1.4426950408889634f;

__device__ __forceinline__ float bf2f_lo(unsigned u) { return __builtin_bit_cast(float, u << 16); }
__device__ __forceinline__ float bf2f_hi(unsigned u) { return __builtin_bit_cast(float, u & 0xffff0000u); }
__device__ __forceinline__ unsigned pack2(float a, float b) { bf16x2 r = {(__bf16)a, (__bf16)b}; return __builtin_bit_cast(unsigned, r); }
__device__ __forceinline__ float rbf(float f) { return bf2f_lo(pack2(f, f)); }
__device__ __forceinline__ float dot2(unsigned a, unsigned b, float acc) {
  return __builtin_amdgcn_fdot2_f32_bf16(__builtin_bit_cast(bf16x2, a), __builtin_bit_cast(bf16x2, b), acc, false);
}
template <int CTRL> __device__ __forceinline__ unsigned dppu(unsigned x) { return __builtin_amdgcn_mov_dpp(x, CTRL, 0xf, 0xf, true); }
template <int CTRL> __device__ __forceinline__ float dppf(float x) { return __builtin_bit_cast(float, dppu<CTRL>(__builtin_bit_cast(unsigned, x))); }
constexpr int XOR1 = 0xB1, XOR2 = 0x4E, HMIRROR = 0x141, ROR1 = 0x121, ROR2 = 0x122, ROR4 = 0x124, ROR8 = 0x128, NEWBCAST = 0x150;
__device__ __forceinline__ float x16_sum(float x) { auto s = __builtin_amdgcn_permlane16_swap(__float_as_uint(x), __float_as_uint(x), false, false); return __uint_as_float(s[0]) + __uint_as_float(s[1]); }
__device__ __forceinline__ float x32_sum(float x) { auto s = __builtin_amdgcn_permlane32_swap(__float_as_uint(x), __float_as_uint(x), false, false); return __uint_as_float(s[0]) + __uint_as_float(s[1]); }
__device__ __forceinline__ float x16_max(float x) { auto s = __builtin_amdgcn_permlane16_swap(__float_as_uint(x), __float_as_uint(x), false, false); return fmaxf(__uint_as_float(s[0]), __uint_as_float(s[1])); }
__device__ __forceinline__ float x32_max(float x) { auto s = __builtin_amdgcn_permlane32_swap(__float_as_uint(x), __float_as_uint(x), false, false); return fmaxf(__uint_as_float(s[0]), __uint_as_float(s[1])); }
__device__ __forceinline__ float wave_sum(float x) {
  x += dppf<ROR8>(x); x += dppf<ROR4>(x); x += dppf<ROR2>(x); x += dppf<ROR1>(x);
  return x32_sum(x16_sum(x));
}
__device__ __forceinline__ float wave_max(float x) {
  x = fmaxf(x, dppf<ROR8>(x)); x = fmaxf(x, dppf<ROR4>(x)); x = fmaxf(x, dppf<ROR2>(x)); x = fmaxf(x, dppf<ROR1>(x));
  return x32_max(x16_max(x));
}
__device__ __forceinline__ f32x4 mfma16(v4u a, v4u b, f32x4 c) {
  return __builtin_amdgcn_mfma_f32_16x16x32_bf16(__builtin_bit_cast(bf16x8, a), __builtin_bit_cast(bf16x8, b), c, 0, 0, 0);
}
__device__ __forceinline__ v2u tr_read(LAS unsigned char* p) { return __builtin_bit_cast(v2u, __builtin_amdgcn_ds_read_tr16_b64_v4i16((LAS v4s*)p)); }
#define LGKM0() asm volatile("s_waitcnt lgkmcnt(0)" ::: "memory")
#define VM0() asm volatile("s_waitcnt vmcnt(0)" ::: "memory")
#define BAR() do { asm volatile("s_waitcnt lgkmcnt(0)" ::: "memory"); __builtin_amdgcn_s_barrier(); asm volatile("" ::: "memory"); } while (0)
#define CFENCE() asm volatile("" ::: "memory")
__device__ __forceinline__ bool all_ok(bool ok) { return __builtin_amdgcn_ballot_w64(!ok) == 0ull; }

__device__ __forceinline__ void ln512(const float (&v)[8], unsigned (&o)[4]) {
  float s = ((v[0] + v[1]) + (v[2] + v[3])) + ((v[4] + v[5]) + (v[6] + v[7]));
  s = wave_sum(s);
  const float mean = s * (1.f / 512.f);
  float q = 0.f;
#pragma unroll
  for (int j = 0; j < 8; ++j) { const float d = v[j] - mean; q = fmaf(d, d, q); }
  q = wave_sum(q);
  const float var = rbf(q * (1.f / 512.f));
  const float mb = rbf(mean);
  const float den = rbf(__builtin_amdgcn_sqrtf(rbf(var + 1e-5f)));
  const float inv = 1.0f / den;
#pragma unroll
  for (int j = 0; j < 4; ++j) o[j] = pack2(rbf(v[2 * j] - mb) * inv, rbf(v[2 * j + 1] - mb) * inv);
}

#define STAMP(i) do {} while (0)

__global__ void __launch_bounds__(NTHR, 3)
fused_decode(const u16* __restrict__ x, const u16* __restrict__ kc, const u16* __restrict__ vc, const u16* __restrict__ wq, const u16* __restrict__ wo,
             const u16* __restrict__ w1, const u16* __restrict__ b1, const u16* __restrict__ w2, const u16* __restrict__ b2, u16* __restrict__ out, unsigned char* ws) {
  extern __shared__ __attribute__((aligned(16))) unsigned char smem[];
  LAS unsigned char* L = (LAS unsigned char*)smem;
  const int tid = threadIdx.x, lane = tid & 63, wave = __builtin_amdgcn_readfirstlane(tid >> 6);
  const int b = blockIdx.x, g = b & 7, j = b >> 3;
  const int head = 4 * g + (j & 3), sp = j >> 2;
  const int pid = head * 8 + sp;
  const int jr = j - 16;
  const bool isT1 = j < 16;
  const int m1 = g * 6 + jr;
  const bool isM1 = (jr >= 0) && (jr < 6) && (m1 < 43);
  const int m2 = g * 8 + (jr - 6);
  const bool isM2 = (jr >= 6) && (jr < 14);
  const __amdgpu_buffer_rsrc_t rs = __builtin_amdgcn_make_buffer_rsrc((void*)ws, 0, WS_END, 0x00020000);
  volatile LAS unsigned* flags = (volatile LAS unsigned*)(L + L_FLAG);
  STAMP(0);
  if (wave == 0) { if (lane < 16) flags[lane] = 0u; }
  BAR();

  if (wave == 0) {
    __builtin_amdgcn_s_setprio(3);
    const unsigned ep = __builtin_amdgcn_raw_buffer_load_b32(rs, WS_EPOCH, 0, AUX_SC1);
    const v4u xv = __builtin_nontemporal_load((const GAS v4u*)x + lane);
    v4u wv[16];
    {
      const GAS unsigned char* p = (const GAS unsigned char*)wq + (size_t)(16 * (lane >> 1)) * 8192 + (head * 128 + 16 * sp + 8 * (lane & 1)) * 2;
#pragma unroll
      for (int i = 0; i < 16; ++i) wv[i] = __builtin_nontemporal_load((const GAS v4u*)(p + i * 8192));
    }
    unsigned bias = 0;
    if (isT1) {
      const GAS unsigned char* p = (const GAS unsigned char*)wo + (size_t)(512 * g + (lane >> 2)) * 1024 + 64 * j + (lane & 3) * 16;
#pragma unroll
      for (int i = 0; i < 32; ++i) __builtin_amdgcn_global_load_lds((const GAS unsigned*)(p + i * 16384), (LAS unsigned*)(L + L_W + i * 1024), 16, 0, AUX_NT);
    } else if (isM1) {
      const GAS unsigned char* p = (const GAS unsigned char*)w1 + (size_t)(lane >> 2) * (FF * 2) + 64 * m1 + (lane & 3) * 16;
#pragma unroll
      for (int i = 0; i < 32; ++i) __builtin_amdgcn_global_load_lds((const GAS unsigned*)(p + i * 16 * (FF * 2)), (LAS unsigned*)(L + L_W + i * 1024), 16, 0, AUX_NT);
      bias = b1[32 * m1 + (lane & 31)];
    } else if (isM2) {
#pragma unroll
      for (int i = 0; i < 22; ++i) {
        const int row = min(64 * i + lane, FF - 1);
        __builtin_amdgcn_global_load_lds((const GAS unsigned*)((const GAS unsigned char*)w2 + (size_t)row * 1024 + 16 * m2), (LAS unsigned*)(L + L_W + i * 1024), 16, 0, AUX_NT);
      }
      bias = b2[8 * m2 + (lane & 7)];
    }
    __builtin_amdgcn_sched_barrier(0);
    float xf[8];
#pragma unroll
    for (int k = 0; k < 4; ++k) { xf[2 * k] = bf2f_lo(xv[k]); xf[2 * k + 1] = bf2f_hi(xv[k]); }
    unsigned xn[4];
    ln512(xf, xn);
    unsigned xa[4], xb[4];
#pragma unroll
    for (int k = 0; k < 4; ++k) { xa[k] = dppu<0xA0>(xn[k]); xb[k] = dppu<0xF5>(xn[k]); }
    float acc[8];
#pragma unroll
    for (int k = 0; k < 8; ++k) acc[k] = 0.f;
#pragma unroll
    for (int i = 0; i < 16; ++i) {
      const unsigned src = (i < 8) ? xa[(i & 7) >> 1] : xb[(i & 7) >> 1];
      const unsigned lo = (i & 1) ? (src >> 16) : (src & 0xffffu), hi = (i & 1) ? (src & 0xffff0000u) : (src << 16);
#pragma unroll
      for (int k = 0; k < 4; ++k) { acc[2 * k] = dot2(wv[i][k], lo, acc[2 * k]); acc[2 * k + 1] = dot2(wv[i][k], hi, acc[2 * k + 1]); }
    }
#pragma unroll
    for (int k = 0; k < 8; ++k) { float a = acc[k]; a += dppf<ROR8>(a); a += dppf<ROR4>(a); a += dppf<ROR2>(a); acc[k] = x32_sum(x16_sum(a)); }
    const unsigned tag = ep + 1u;
    {
      const unsigned q0 = pack2(acc[0], acc[1]), q1 = pack2(acc[2], acc[3]), q2 = pack2(acc[4], acc[5]), q3 = pack2(acc[6], acc[7]);
      const int sub = (lane >> 1) & 1, half = lane & 1;
      v4u gq = {sub ? q2 : q0, tag, sub ? q3 : q1, tag};
      if (lane < 4) __builtin_amdgcn_raw_buffer_store_b128(gq, rs, WS_QG + head * 512 + (sp * 4 + 2 * half + sub) * 16, 0, AUX_SC1);
    }
    STAMP(1);
    {
      v4u gq;
      for (unsigned spin = 0;; ++spin) {
        CFENCE();
        gq = __builtin_amdgcn_raw_buffer_load_b128(rs, WS_QG + head * 512 + (lane & 31) * 16, 0, AUX_SC1);
        if (all_ok(gq.y == tag && gq.w == tag) || spin > SPIN_CAP) break;
      }
      if (lane < 32) *(LAS v2u*)(L + L_Q + lane * 8) = v2u{gq.x, gq.z};
      if (lane == 0) flags[1] = tag;
      LGKM0();
      if (lane == 0) flags[0] = 1u;
    }
    STAMP(2);
    VM0();
    BAR();
    STAMP(3);
    {
      const f32x2 ml = *(LAS f32x2*)(L + L_ML + (lane & 7) * 8);
      float M = ml.x;
      M = fmaxf(M, dppf<XOR1>(M)); M = fmaxf(M, dppf<XOR2>(M)); M = fmaxf(M, dppf<HMIRROR>(M));
      const float f = __builtin_amdgcn_exp2f((ml.x - M) * LOG2E);
      float Ls = f * ml.y;
      Ls += dppf<XOR1>(Ls); Ls += dppf<XOR2>(Ls); Ls += dppf<HMIRROR>(Ls);
      float o0 = 0.f, o1 = 0.f;
#pragma unroll
      for (int u = 0; u < 8; ++u) {
        const float fu = __builtin_bit_cast(float, __builtin_amdgcn_readlane(__builtin_bit_cast(int, f), u));
        const f32x2 ov = *(LAS f32x2*)(L + L_O + u * 512 + lane * 8);
        o0 = fmaf(fu, ov.x, o0); o1 = fmaf(fu, ov.y, o1);
      }
      v4u go = {__float_as_uint(o0), tag, __float_as_uint(o1), tag};
      __builtin_amdgcn_raw_buffer_store_b128(go, rs, WS_XO + pid * 1024 + lane * 16, 0, AUX_SC1);
      v4u gm = {__float_as_uint(M), tag, __float_as_uint(Ls), tag};
      if (lane == 0) __builtin_amdgcn_raw_buffer_store_b128(gm, rs, WS_XM + pid * 16, 0, AUX_SC1);
    }
    STAMP(4);
    if (isT1) {
      BAR();
      if (lane < 16) {
        float y0 = 0.f, y1 = 0.f;
#pragma unroll
        for (int u = 0; u < 8; ++u) { const f32x2 r = *(LAS f32x2*)(L + L_RED + u * 128 + lane * 8); y0 += r.x; y1 += r.y; }
        v4u gy = {__float_as_uint(y0), tag, __float_as_uint(y1), tag};
        __builtin_amdgcn_raw_buffer_store_b128(gy, rs, WS_GG + g * 4096 + (16 * j + lane) * 16, 0, AUX_SC1);
      }
      STAMP(5);
    } else if (isM1) {
      BAR();
      BAR();
      float a = 0.f;
#pragma unroll
      for (int u = 0; u < 8; ++u) a += *(LAS float*)(L + L_RED + u * 128 + (lane & 31) * 4);
      a = rbf(rbf(a) + bf2f_lo(bias));
      const float t = 0.7978845608028654f * (a + 0.044715f * a * a * a);
      const float e = __builtin_amdgcn_exp2f(t * (2.f * LOG2E));
      const float th = 1.f - 2.f / (e + 1.f);
      const float ge = 0.5f * a * (1.f + th);
      const float gn = dppf<XOR1>(ge);
      const unsigned pr = pack2(ge, gn);
      const unsigned pr2 = dppu<XOR2>(pr);
      v4u gh = {pr, tag, pr2, tag};
      if (lane < 32 && (lane & 3) == 0) __builtin_amdgcn_raw_buffer_store_b128(gh, rs, WS_H1 + (8 * m1 + (lane >> 2)) * 16, 0, AUX_SC1);
      STAMP(5);
    } else if (isM2) {
      const unsigned xw = ((const GAS unsigned*)x)[4 * m2 + (lane & 3)];
      v4u gy;
      for (unsigned spin = 0;; ++spin) {
        CFENCE();
        gy = __builtin_amdgcn_raw_buffer_load_b128(rs, WS_GG + ((lane >> 2) & 7) * 4096 + (4 * m2 + (lane & 3)) * 16, 0, AUX_SC1);
        if (all_ok(gy.y == tag && gy.w == tag) || spin > SPIN_CAP) break;
      }
      float y0 = __uint_as_float(gy.x), y1 = __uint_as_float(gy.z);
      y0 += dppf<ROR4>(y0); y0 += dppf<ROR8>(y0); y0 = x16_sum(y0);
      y1 += dppf<ROR4>(y1); y1 += dppf<ROR8>(y1); y1 = x16_sum(y1);
      const float h0 = rbf(bf2f_lo(xw) + rbf(y0)), h1 = rbf(bf2f_hi(xw) + rbf(y1));
      if (lane < 4) *(LAS f32x2*)(L + L_H + lane * 8) = f32x2{h0, h1};
      BAR();
      float a = 0.f;
#pragma unroll
      for (int u = 0; u < 8; ++u) a += *(LAS float*)(L + L_RED + u * 128 + (lane & 7) * 4);
      a = rbf(rbf(a) + bf2f_lo(bias));
      const float hv = *(LAS float*)(L + L_H + (lane & 7) * 4);
      const float r = hv + a;
      const float rn = dppf<XOR1>(r);
      const unsigned pr = pack2(r, rn);
      if (lane < 8 && (lane & 1) == 0) ((GAS unsigned*)out)[4 * m2 + (lane >> 1)] = pr;
      if (m2 == 0 && lane == 0) __builtin_amdgcn_raw_buffer_store_b32(tag, rs, WS_EPOCH, 0, AUX_SC1);
      STAMP(5);
    }
  } else {
    const int wv = wave - 1;
    const size_t base = ((size_t)head * SK + (SK - WIN) + sp * 512 + wv * 64) * (DH * 2);
    const __amdgpu_buffer_rsrc_t rk = __builtin_amdgcn_make_buffer_rsrc((void*)((const char*)kc + base), 0, 16384, 0x00020000);
    const __amdgpu_buffer_rsrc_t rv = __builtin_amdgcn_make_buffer_rsrc((void*)((const char*)vc + base), 0, 16384, 0x00020000);
    const int rot = b * 5 + (b >> 3) * 3;
    v4u K[16], V[16];
#pragma unroll
    for (int i = 0; i < 16; ++i) K[i] = __builtin_amdgcn_raw_buffer_load_b128(rk, lane * 16, ((i + rot) & 15) * 1024, AUX_NT);
#pragma unroll
    for (int i = 0; i < 16; ++i) V[i] = __builtin_amdgcn_raw_buffer_load_b128(rv, lane * 16, ((i + rot) & 15) * 1024, AUX_NT);
    __builtin_amdgcn_sched_barrier(0);
    STAMP(1);
    for (unsigned spin = 0; flags[0] == 0u && spin < (SPIN_CAP << 4); ++spin) __builtin_amdgcn_s_sleep(2);
    CFENCE();
    const unsigned tag = flags[1];
    const v4u qv = *(LAS v4u*)(L + L_Q + (lane & 15) * 16);
    STAMP(2);
    float d[16];
#pragma unroll
    for (int i = 0; i < 16; ++i) {
      float a = dot2(K[i].x, qv.x, 0.f); a = dot2(K[i].y, qv.y, a); a = dot2(K[i].z, qv.z, a); a = dot2(K[i].w, qv.w, a);
      d[i] = a;
    }
    const int c = lane & 15;
#define TR_STEP(HALF, CTRL, BIT) _Pragma("unroll") for (int hh = 0; hh < (HALF); ++hh) { \
      const float lo_ = d[hh], hi_ = d[hh + (HALF)]; const float t1_ = lo_ + dppf<CTRL>(lo_), t2_ = hi_ + dppf<CTRL>(hi_); d[hh] = (c & (BIT)) ? t2_ : t1_; }
    TR_STEP(8, ROR8, 8) TR_STEP(4, HMIRROR, 4) TR_STEP(2, XOR2, 2) TR_STEP(1, XOR1, 1)
#undef TR_STEP
    const float s = rbf(rbf(d[0]) * 0.08838834764831845f);
    const float m = wave_max(s);
    const float p = __builtin_amdgcn_exp2f((s - m) * LOG2E);
    const float l = wave_sum(p);
    if (lane == 0) *(LAS f32x2*)(L + L_ML + wv * 8) = f32x2{m, l};
    const unsigned pp = pack2(p, p);
    unsigned mask[4];
#pragma unroll
    for (int t = 0; t < 4; ++t) mask[t] = (c < 8 && (c >> 1) == t) ? ((c & 1) ? 0xffff0000u : 0x0000ffffu) : 0u;
    STAMP(3);
    f32x4 acc = {0.f, 0.f, 0.f, 0.f};
#define PV_STEP(i) { const unsigned pb = __builtin_amdgcn_update_dpp(0u, pp, NEWBCAST + (i), 0xf, 0xf, true); \
      v4u a = {pb & mask[0], pb & mask[1], pb & mask[2], pb & mask[3]}; acc = mfma16(a, V[i], acc); }
    PV_STEP(0) PV_STEP(1) PV_STEP(2) PV_STEP(3) PV_STEP(4) PV_STEP(5) PV_STEP(6) PV_STEP(7)
    PV_STEP(8) PV_STEP(9) PV_STEP(10) PV_STEP(11) PV_STEP(12) PV_STEP(13) PV_STEP(14) PV_STEP(15)
#undef PV_STEP
    if (lane < 32) *(LAS f32x4*)(L + L_O + wv * 512 + (8 * c + 4 * (lane >> 4)) * 4) = acc;
    STAMP(4);
    BAR();
    LAS unsigned char* scr = L + L_SCR + wv * 512;
    const int g4 = lane >> 4;
    if (isT1) {
      const int hh = wv >> 1, dh = wv & 1, jj = lane & 31, sh = lane >> 5;
      const int xo = WS_XO + ((4 * g + hh) * 8 + 4 * sh) * 1024 + (32 * dh + jj) * 16;
      const int xm = WS_XM + ((4 * g + hh) * 8 + (lane & 7)) * 16;
      v4u go[4], gm;
      v4u Bf[2][2];
      bool first = true;
      for (unsigned spin = 0;; ++spin) {
        CFENCE();
#pragma unroll
        for (int k = 0; k < 4; ++k) go[k] = __builtin_amdgcn_raw_buffer_load_b128(rs, xo + k * 1024, 0, AUX_SC1);
        gm = __builtin_amdgcn_raw_buffer_load_b128(rs, xm, 0, AUX_SC1);
        if (first) {
          first = false;
          __builtin_amdgcn_sched_barrier(0);
#pragma unroll
          for (int t = 0; t < 2; ++t)
#pragma unroll
            for (int cb = 0; cb < 2; ++cb) {
              LAS unsigned char* a0 = L + L_W + (64 * wv + 32 * t + 8 * g4 + ((lane & 15) >> 2)) * 64 + (16 * cb + 4 * (lane & 3)) * 2;
              const v2u lo = tr_read(a0), hi = tr_read(a0 + 4 * 64);
              Bf[t][cb] = v4u{lo.x, lo.y, hi.x, hi.y};
            }
          __builtin_amdgcn_sched_barrier(0);
        }
        bool ok = gm.y == tag && gm.w == tag;
#pragma unroll
        for (int k = 0; k < 4; ++k) ok = ok && go[k].y == tag && go[k].w == tag;
        if (all_ok(ok) || spin > SPIN_CAP) break;
      }
      STAMP(5);
      const float mm = __uint_as_float(gm.x), ll = __uint_as_float(gm.z);
      float M = mm;
      M = fmaxf(M, dppf<XOR1>(M)); M = fmaxf(M, dppf<XOR2>(M)); M = fmaxf(M, dppf<HMIRROR>(M));
      const float f = __builtin_amdgcn_exp2f((mm - M) * LOG2E);
      float Ls = f * ll;
      Ls += dppf<XOR1>(Ls); Ls += dppf<XOR2>(Ls); Ls += dppf<HMIRROR>(Ls);
      const float fn = f / Ls;
      float o0 = 0.f, o1 = 0.f;
#pragma unroll
      for (int k = 0; k < 4; ++k) {
        const float fa = __builtin_bit_cast(float, __builtin_amdgcn_readlane(__builtin_bit_cast(int, fn), k));
        const float fb = __builtin_bit_cast(float, __builtin_amdgcn_readlane(__builtin_bit_cast(int, fn), 4 + k));
        const float fk = sh ? fb : fa;
        o0 = fmaf(fk, __uint_as_float(go[k].x), o0); o1 = fmaf(fk, __uint_as_float(go[k].z), o1);
      }
      o0 = x32_sum(o0); o1 = x32_sum(o1);
      if (lane < 32) *(LAS unsigned*)(scr + jj * 4) = pack2(o0, o1);
      LGKM0();
      const v4u A0 = *(LAS v4u*)(scr + (4 * g4) * 4), A1 = *(LAS v4u*)(scr + (16 + 4 * g4) * 4);
      f32x4 D0 = {0.f, 0.f, 0.f, 0.f}, D1 = {0.f, 0.f, 0.f, 0.f};
      D0 = mfma16(A0, Bf[0][0], D0); D1 = mfma16(A0, Bf[0][1], D1);
      D0 = mfma16(A1, Bf[1][0], D0); D1 = mfma16(A1, Bf[1][1], D1);
      if (lane < 32) *(LAS float*)(L + L_RED + wv * 128 + lane * 4) = (lane < 16) ? D0[0] : D1[0];
      BAR();
    } else if (isM1) {
      const v4u xv = __builtin_nontemporal_load((const GAS v4u*)x + lane);
      const int tt = wv * 64 + lane, pj = tt & 255, half = tt >> 8;
      const int yo = WS_GG + (4 * half) * 4096 + pj * 16;
      v4u gy[4];
      v4u Bf[2][2];
      bool first = true;
      for (unsigned spin = 0;; ++spin) {
        CFENCE();
#pragma unroll
        for (int k = 0; k < 4; ++k) gy[k] = __builtin_amdgcn_raw_buffer_load_b128(rs, yo + k * 4096, 0, AUX_SC1);
        if (first) {
          first = false;
          __builtin_amdgcn_sched_barrier(0);
#pragma unroll
          for (int t = 0; t < 2; ++t)
#pragma unroll
            for (int cb = 0; cb < 2; ++cb) {
              LAS unsigned char* a0 = L + L_W + (64 * wv + 32 * t + 8 * g4 + ((lane & 15) >> 2)) * 64 + (16 * cb + 4 * (lane & 3)) * 2;
              const v2u lo = tr_read(a0), hi = tr_read(a0 + 4 * 64);
              Bf[t][cb] = v4u{lo.x, lo.y, hi.x, hi.y};
            }
          __builtin_amdgcn_sched_barrier(0);
        }
        bool ok = true;
#pragma unroll
        for (int k = 0; k < 4; ++k) ok = ok && gy[k].y == tag && gy[k].w == tag;
        if (all_ok(ok) || spin > SPIN_CAP) break;
      }
      STAMP(5);
      {
        const float y0 = ((__uint_as_float(gy[0].x) + __uint_as_float(gy[1].x)) + __uint_as_float(gy[2].x)) + __uint_as_float(gy[3].x);
        const float y1 = ((__uint_as_float(gy[0].z) + __uint_as_float(gy[1].z)) + __uint_as_float(gy[2].z)) + __uint_as_float(gy[3].z);
        *(LAS f32x2*)(L + L_YP + half * 2048 + pj * 8) = f32x2{y0, y1};
      }
      BAR();
      float hv[8];
      {
        const f32x4 a0 = *(LAS f32x4*)(L + L_YP + lane * 32), a1 = *(LAS f32x4*)(L + L_YP + lane * 32 + 16);
        const f32x4 c0 = *(LAS f32x4*)(L + L_YP + 2048 + lane * 32), c1 = *(LAS f32x4*)(L + L_YP + 2048 + lane * 32 + 16);
#pragma unroll
        for (int k = 0; k < 4; ++k) {
          const float ya = (k < 2 ? a0[2 * k] : a1[2 * k - 4]) + (k < 2 ? c0[2 * k] : c1[2 * k - 4]);
          const float yb = (k < 2 ? a0[2 * k + 1] : a1[2 * k - 3]) + (k < 2 ? c0[2 * k + 1] : c1[2 * k - 3]);
          hv[2 * k] = rbf(bf2f_lo(xv[k]) + rbf(ya)); hv[2 * k + 1] = rbf(bf2f_hi(xv[k]) + rbf(yb));
        }
      }
      unsigned hn[4];
      ln512(hv, hn);
      if ((lane >> 3) == wv) *(LAS v4u*)(scr + (lane & 7) * 16) = v4u{hn[0], hn[1], hn[2], hn[3]};
      LGKM0();
      const v4u A0 = *(LAS v4u*)(scr + (4 * g4) * 4), A1 = *(LAS v4u*)(scr + (16 + 4 * g4) * 4);
      f32x4 D0 = {0.f, 0.f, 0.f, 0.f}, D1 = {0.f, 0.f, 0.f, 0.f};
      D0 = mfma16(A0, Bf[0][0], D0); D1 = mfma16(A0, Bf[0][1], D1);
      D0 = mfma16(A1, Bf[1][0], D0); D1 = mfma16(A1, Bf[1][1], D1);
      if (lane < 32) *(LAS float*)(L + L_RED + wv * 128 + lane * 4) = (lane < 16) ? D0[0] : D1[0];
      BAR();
    } else if (isM2) {
      const int t0 = wv * 5 + min(wv, 3), nt = wv < 3 ? 6 : 5;
      const int ho = WS_H1 + (8 * t0 + min(lane, 8 * nt - 1)) * 16;
      v4u gh;
      v4u Bf[6];
      bool first = true;
      for (unsigned spin = 0;; ++spin) {
        CFENCE();
        gh = __builtin_amdgcn_raw_buffer_load_b128(rs, ho, 0, AUX_SC1);
        if (first) {
          first = false;
          __builtin_amdgcn_sched_barrier(0);
#pragma unroll
          for (int u = 0; u < 6; ++u) {
            const int t = min(t0 + u, 42);
            LAS unsigned char* a0 = L + L_W + (32 * t + 8 * g4 + ((lane & 15) >> 2)) * 16 + (lane & 1) * 8;
            const v2u lo = tr_read(a0), hi = tr_read(a0 + 4 * 16);
            Bf[u] = v4u{lo.x, lo.y, hi.x, hi.y};
          }
          __builtin_amdgcn_sched_barrier(0);
        }
        if (all_ok(gh.y == tag && gh.w == tag) || spin > SPIN_CAP) break;
      }
      STAMP(5);
      *(LAS v2u*)(scr + lane * 8) = v2u{gh.x, gh.z};
      LGKM0();
      f32x4 D0 = {0.f, 0.f, 0.f, 0.f};
#pragma unroll
      for (int u = 0; u < 6; ++u) {
        if (u < nt) { const v4u A = *(LAS v4u*)(scr + (8 * u + 2 * g4) * 8); D0 = mfma16(A, Bf[u], D0); }
      }
      if (lane < 8) *(LAS float*)(L + L_RED + wv * 128 + lane * 4) = D0[0];
      BAR();
    }
    STAMP(6);
  }
}
}

extern "C" void kernel_launch(void* const* d_in, const int* in_sizes, int n_in, void* d_out, int out_size, void* d_ws, size_t ws_size, hipStream_t stream) {
  (void)in_sizes; (void)n_in; (void)out_size; (void)ws_size;
  hipFuncSetAttribute((const void*)fused_decode, hipFuncAttributeMaxDynamicSharedMemorySize, LDS_BYTES);
  hipLaunchKernelGGL(fused_decode, dim3(NWG), dim3(NTHR), LDS_BYTES, stream,
                     (const u16*)d_in[0], (const u16*)d_in[1], (const u16*)d_in[2], (const u16*)d_in[3], (const u16*)d_in[4],
                     (const u16*)d_in[6], (const u16*)d_in[7], (const u16*)d_in[8], (const u16*)d_in[9], (u16*)d_out, (unsigned char*)d_ws);
}
```

```cpp
#include <hip/hip_runtime.h>
#include <stdint.h>

typedef unsigned v4u __attribute__((ext_vector_type(4)));
typedef unsigned v2u __attribute__((ext_vector_type(2)));
typedef float f32x4 __attribute__((ext_vector_type(4)));
typedef float f32x2 __attribute__((ext_vector_type(2)));
typedef short v4s __attribute__((ext_vector_type(4)));
typedef __bf16 bf16x2 __attribute__((ext_vector_type(2)));
typedef __bf16 bf16x8 __attribute__((ext_vector_type(8)));
typedef unsigned short u16;
#define LAS __attribute__((address_space(3)))
#define GAS __attribute__((address_space(1)))

#define WQ_SEG 32
#define WINW 4
#define VSPLIT 2
#define ISSN 8
#define T1_WAIT 0
#define M1_WAIT 1
#define THRN 12
#define T1_SLEEP 4
#define M1_GATE 0
#define M1_DELAY 12
#define F_GATE 1
#define F_DELAY 4
#define DPRIO0 2
#define DPRIO1 0
#define MPRIO0 0
#define MPRIO1 0
#define NUTAB 1
#define M1_LATE 1
#define DWIN 4
#define DTHRN 12
#define W1_AUX 2
#define WQ_AUX 2
namespace {
constexpr int SK = 16384, DH = 128, WIN = 4096, FF = 1376;
#define NWAVES 10
constexpr int NWG = 256, NTHR = 64 * NWAVES;
constexpr int WS_EPOCH = 0;
constexpr int WS_XM = 4096;
constexpr int WS_FL = 8192;
constexpr int WS_QG = 12288;
constexpr int WS_XO = WS_QG + 131072;
constexpr int WS_GG = WS_XO + 262144;
constexpr int WS_P2 = WS_GG + 65536;
constexpr int WS_END = WS_P2 + 44 * 4096;
constexpr int WS_STAMP = 1 << 20;
static_assert(WS_END <= WS_STAMP, "ws");
constexpr int L_W = 0;
constexpr int L_Q = 65536;
constexpr int L_FLAG = L_Q + 256;
constexpr int L_ML = L_FLAG + 128;
constexpr int L_O = L_ML + 64;
constexpr int L_RED = L_O + 4608;
constexpr int L_ST = L_RED + 1024;
constexpr int L_END = L_ST + 128;
constexpr int LDS_BYTES = 84 * 1024;
static_assert(L_END <= LDS_BYTES, "lds");
constexpr int F_Q = 0, F_TAG = 1, F_CNT1 = 2, F_W = 4, F_GO = 5, F_PUB = 6, F_MAX = 8;
constexpr unsigned SENT = 0x7fffffffu;
constexpr int OOB = 0x7ffffff0;
constexpr int AUX_NT = 2, AUX_SC1 = 16;
constexpr unsigned SPIN_CAP = 1u << 16;
constexpr float LOG2E = 1.4426950408889634f;

__device__ __forceinline__ float bf2f_lo(unsigned u) { return __builtin_bit_cast(float, u << 16); }
__device__ __forceinline__ float bf2f_hi(unsigned u) { return __builtin_bit_cast(float, u & 0xffff0000u); }
__device__ __forceinline__ unsigned pack2(float a, float b) { bf16x2 r = {(__bf16)a, (__bf16)b}; return __builtin_bit_cast(unsigned, r); }
__device__ __forceinline__ float rbf(float f) { return bf2f_lo(pack2(f, f)); }
__device__ __forceinline__ float dot2(unsigned a, unsigned b, float acc) {
  return __builtin_amdgcn_fdot2_f32_bf16(__builtin_bit_cast(bf16x2, a), __builtin_bit_cast(bf16x2, b), acc, false);
}
template <int CTRL> __device__ __forceinline__ unsigned dppu(unsigned x) { return __builtin_amdgcn_mov_dpp(x, CTRL, 0xf, 0xf, true); }
template <int CTRL> __device__ __forceinline__ float dppf(float x) { return __builtin_bit_cast(float, dppu<CTRL>(__builtin_bit_cast(unsigned, x))); }
constexpr int XOR1 = 0xB1, XOR2 = 0x4E, HMIRROR = 0x141, ROR1 = 0x121, ROR2 = 0x122, ROR4 = 0x124, ROR8 = 0x128, NEWBCAST = 0x150;
__device__ __forceinline__ float x16_sum(float x) { auto s = __builtin_amdgcn_permlane16_swap(__float_as_uint(x), __float_as_uint(x), false, false); return __uint_as_float(s[0]) + __uint_as_float(s[1]); }
__device__ __forceinline__ float x32_sum(float x) { auto s = __builtin_amdgcn_permlane32_swap(__float_as_uint(x), __float_as_uint(x), false, false); return __uint_as_float(s[0]) + __uint_as_float(s[1]); }
__device__ __forceinline__ float x16_max(float x) { auto s = __builtin_amdgcn_permlane16_swap(__float_as_uint(x), __float_as_uint(x), false, false); return fmaxf(__uint_as_float(s[0]), __uint_as_float(s[1])); }
__device__ __forceinline__ float x32_max(float x) { auto s = __builtin_amdgcn_permlane32_swap(__float_as_uint(x), __float_as_uint(x), false, false); return fmaxf(__uint_as_float(s[0]), __uint_as_float(s[1])); }
__device__ __forceinline__ float wave_sum(float x) {
  x += dppf<ROR8>(x); x += dppf<ROR4>(x); x += dppf<ROR2>(x); x += dppf<ROR1>(x);
  return x32_sum(x16_sum(x));
}
__device__ __forceinline__ float wave_max(float x) {
  x = fmaxf(x, dppf<ROR8>(x)); x = fmaxf(x, dppf<ROR4>(x)); x = fmaxf(x, dppf<ROR2>(x)); x = fmaxf(x, dppf<ROR1>(x));
  return x32_max(x16_max(x));
}
__device__ __forceinline__ f32x4 mfma16(v4u a, v4u b, f32x4 c) {
  return __builtin_amdgcn_mfma_f32_16x16x32_bf16(__builtin_bit_cast(bf16x8, a), __builtin_bit_cast(bf16x8, b), c, 0, 0, 0);
}
__device__ __forceinline__ v2u tr_read(LAS unsigned char* p) { return __builtin_bit_cast(v2u, __builtin_amdgcn_ds_read_tr16_b64_v4i16((LAS v4s*)p)); }
#define LGKM0() asm volatile("s_waitcnt lgkmcnt(0)" ::: "memory")
#define VM0() asm volatile("s_waitcnt vmcnt(0)" ::: "memory")
#define BAR() do { asm volatile("s_waitcnt lgkmcnt(0)" ::: "memory"); __builtin_amdgcn_s_barrier(); asm volatile("" ::: "memory"); } while (0)
#define CFENCE() asm volatile("" ::: "memory")
__device__ __forceinline__ bool all_ok(bool ok) { return __builtin_amdgcn_ballot_w64(!ok) == 0ull; }

__device__ __forceinline__ void ln_stats(const float (&v)[8], float& mb, float& inv) {
  const float c = __builtin_bit_cast(float, __builtin_amdgcn_readfirstlane(__builtin_bit_cast(int, v[0])));
  float s = 0.f, q = 0.f;
#pragma unroll
  for (int j = 0; j < 8; ++j) { const float d = v[j] - c; s += d; q = fmaf(d, d, q); }
  s += dppf<ROR8>(s); q += dppf<ROR8>(q); s += dppf<ROR4>(s); q += dppf<ROR4>(q);
  s += dppf<ROR2>(s); q += dppf<ROR2>(q); s += dppf<ROR1>(s); q += dppf<ROR1>(q);
  s = x16_sum(s); q = x16_sum(q); s = x32_sum(s); q = x32_sum(q);
  const float ms = s * (1.f / 512.f);
  const float var = rbf(fmaxf(fmaf(-ms, ms, q * (1.f / 512.f)), 0.f));
  mb = rbf(c + ms);
  const float den = rbf(__builtin_amdgcn_sqrtf(rbf(var + 1e-5f)));
  inv = __builtin_amdgcn_rcpf(den);
}
__device__ __forceinline__ float rcp(float x) { return __builtin_amdgcn_rcpf(x); }
__device__ __forceinline__ unsigned ln_apply(unsigned pr, float mb, float inv) { return pack2(rbf(bf2f_lo(pr) - mb) * inv, rbf(bf2f_hi(pr) - mb) * inv); }
__device__ __forceinline__ float sum8(float x) { x += dppf<XOR1>(x); x += dppf<XOR2>(x); x += dppf<HMIRROR>(x); return x; }
__device__ __forceinline__ float max8(float x) { x = fmaxf(x, dppf<XOR1>(x)); x = fmaxf(x, dppf<XOR2>(x)); x = fmaxf(x, dppf<HMIRROR>(x)); return x; }
template <int N> __device__ __forceinline__ void gather_pass(__amdgpu_buffer_rsrc_t rs, v4u (&t)[N], const int (&off)[N]) {
  CFENCE();
#pragma unroll
  for (int k = 0; k < N; ++k) t[k] = __builtin_amdgcn_raw_buffer_load_b128(rs, off[k], 0, AUX_SC1);
}
template <int N> __device__ __forceinline__ bool gather_check(const v4u (&t)[N], v2u (&g)[N], int (&off)[N], unsigned tag) {
  bool all = true;
#pragma unroll
  for (int k = 0; k < N; ++k) {
    const bool live = off[k] != OOB;
    const bool ok = live && t[k].y == tag && t[k].w == tag;
    g[k].x = ok ? t[k].x : g[k].x; g[k].y = ok ? t[k].z : g[k].y;
    off[k] = ok ? OOB : off[k];
    all = all && (ok || !live);
  }
  return all_ok(all);
}
template <int N, int SLEEP = 0> __device__ __forceinline__ void gather(__amdgpu_buffer_rsrc_t rs, v2u (&g)[N], int (&off)[N], unsigned tag) {
  for (unsigned spin = 0;; ++spin) {
    v4u t[N];
    gather_pass<N>(rs, t, off);
    if (gather_check<N>(t, g, off, tag) || spin > SPIN_CAP) break;
    if (SLEEP) __builtin_amdgcn_s_sleep(SLEEP);
  }
}
__device__ __forceinline__ void lds_wait(volatile LAS unsigned* f) {
  for (unsigned spin = 0; *f == 0u && spin < (SPIN_CAP << 4); ++spin) __builtin_amdgcn_s_sleep(1);
  CFENCE();
}
__device__ __forceinline__ unsigned lds_inc(LAS unsigned char* L, int w) {
  unsigned r = 0;
  asm volatile("" ::: "memory");
  if ((threadIdx.x & 63) == 0) r = __hip_atomic_fetch_add((LAS unsigned*)(L + L_FLAG + w * 4), 1u, __ATOMIC_RELAXED, __HIP_MEMORY_SCOPE_WORKGROUP);
  r = __builtin_amdgcn_readfirstlane(r);
  asm volatile("" ::: "memory");
  return r;
}

template <bool NT> __device__ __forceinline__ void dma16(__amdgpu_buffer_rsrc_t rs, int voff, int soff, unsigned lds) {
  if (NT) asm volatile("s_mov_b32 m0, %0\n\ts_nop 0\n\tbuffer_load_dwordx4 %1, %2, %3 offen nt lds" :: "s"(lds), "v"(voff), "s"(rs), "s"(soff) : "m0", "memory");
  else asm volatile("s_mov_b32 m0, %0\n\ts_nop 0\n\tbuffer_load_dwordx4 %1, %2, %3 offen lds" :: "s"(lds), "v"(voff), "s"(rs), "s"(soff) : "m0", "memory");
}

__device__ __forceinline__ void weight_job(int job, LAS unsigned char* L, const u16* wo, const u16* w1, const u16* w2, int g, int j, int m1, int lane) {
  const bool isT1 = job == 0, isw1 = job == 1;
  const int tl = ((lane >> 2) & 7), tb = (lane >> 5) * 64 + (lane & 3) * 16;
  const void* wp = isT1 ? (const void*)wo : isw1 ? (const void*)w1 : (const void*)w2;
  const __amdgpu_buffer_rsrc_t rw = __builtin_amdgcn_make_buffer_rsrc((void*)wp, 0, isT1 ? 4096 * 1024 : FF * 1024, 0x00020000);
  const int vo = isT1 ? ((4 * g + 2 * (j & 1)) * 128 + tl) * 1024 + 128 * (j >> 1) + tb
               : isw1 ? (lane >> 2) * (FF * 2) + 64 * m1 + (lane & 3) * 16 : (32 * m1 + tl) * 1024 + tb;
  const int s1 = isT1 ? 8192 : isw1 ? 16 * FF * 2 : 8192, s4 = isT1 ? 4 * 8192 : isw1 ? 4 * 16 * FF * 2 : 128;
  const unsigned lb = (unsigned)(uintptr_t)(L + L_W) + (job == 2 ? 32768 : 0);
#pragma unroll
  for (int i = 0; i < 32; ++i) {
    if (i == 0) { if (isT1) __builtin_amdgcn_s_setprio(DPRIO0); else __builtin_amdgcn_s_setprio(MPRIO0); }
    if (i >= DWIN && i < DTHRN && (isT1 || !M1_LATE)) { __builtin_amdgcn_sched_barrier(0); asm volatile("s_waitcnt vmcnt(%0)" :: "n"(DWIN - 1)); __builtin_amdgcn_sched_barrier(0); }
    if (i == 16) { if (isT1) __builtin_amdgcn_s_setprio(DPRIO1); else __builtin_amdgcn_s_setprio(MPRIO1); }
    dma16<true>(rw, vo, (i & 3) * s1 + (i >> 2) * s4, lb + i * 1024);
  }
}

#define STAMP_DECL
#define STAMP(i) do {} while (0)
#define STAMP_FLUSH() do {} while (0)

__global__ void __launch_bounds__(NTHR, 3)
fused_decode(const u16* __restrict__ x, const u16* __restrict__ kc, const u16* __restrict__ vc, const u16* __restrict__ wq, const u16* __restrict__ wo,
             const u16* __restrict__ w1, const u16* __restrict__ b1, const u16* __restrict__ w2, const u16* __restrict__ b2, u16* __restrict__ out, unsigned char* ws) {
  extern __shared__ __attribute__((aligned(16))) unsigned char smem[];
  LAS unsigned char* L = (LAS unsigned char*)smem;
  const int tid = threadIdx.x, lane = tid & 63, wave = __builtin_amdgcn_readfirstlane(tid >> 6);
  const int b = blockIdx.x, g = b & 7, j = b >> 3;
  const int head = 4 * g + (j & 3), sp = j >> 2;
  const int pid = head * 8 + sp;
  const bool rlo = (j & 3) < 2;
  const int nu = sp < 4 ? 8 : sp == 4 ? 7 : sp == 5 ? (rlo ? 7 : 8) : sp == 6 ? (rlo ? 9 : 8) : 9;
  const int u0 = sp <= 4 ? 8 * sp : sp == 5 ? 39 : sp == 6 ? (rlo ? 46 : 47) : 55;
  const int jr = j - 16;
  const bool isT1 = j < 16;
  const int m1 = g * 6 + jr;
  const bool isM1 = (jr >= 0) && (jr < 6) && (m1 < 43);
  const int m2 = g * 8 + (jr - 6);
  const bool isF = (jr >= 6) && (jr < 14);
  const __amdgpu_buffer_rsrc_t rs = __builtin_amdgcn_make_buffer_rsrc((void*)ws, 0, WS_END, 0x00020000);
  volatile LAS unsigned* flags = (volatile LAS unsigned*)(L + L_FLAG);
  STAMP_DECL
  STAMP(0);

  if (wave == 0) {
    __builtin_amdgcn_s_setprio(3);
    const __amdgpu_buffer_rsrc_t rx = __builtin_amdgcn_make_buffer_rsrc((void*)x, 0, 1024, 0x00020000);
    const v4u xv = __builtin_amdgcn_raw_buffer_load_b128(rx, lane * 16, 0, 0);
    __builtin_amdgcn_sched_barrier(0);
    v4u wv[16];
    const int rq = sp >> 1, ch = sp & 1;
    {
      const __amdgpu_buffer_rsrc_t rw = __builtin_amdgcn_make_buffer_rsrc((void*)wq, 0, 512 * 8192, 0x00020000);
      const int vo = (rq * 128 + (lane >> 3)) * 8192 + (head * 128 + 64 * ch + 8 * (lane & 7)) * 2;
#pragma unroll
      for (int i = 0; i < 16; ++i) wv[i] = __builtin_amdgcn_raw_buffer_load_b128(rw, vo, i * 8 * 8192, AUX_NT);
    }
    if (lane < 48) flags[lane] = (lane >= F_MAX && lane < F_MAX + 16) ? (lane < F_MAX + nu ? SENT : 0xff800000u) : 0u;
    __builtin_amdgcn_sched_barrier(0);
    BAR();
    __builtin_amdgcn_sched_barrier(0);
    const unsigned ep = *(const __attribute__((address_space(4))) unsigned*)(ws + WS_EPOCH);
    STAMP(7);
    float xf[8];
#pragma unroll
    for (int k = 0; k < 4; ++k) { xf[2 * k] = bf2f_lo(xv[k]); xf[2 * k + 1] = bf2f_hi(xv[k]); }
    float mb, inv;
    ln_stats(xf, mb, inv);
    {
      LAS u16* T = (LAS u16*)(L + L_O + (lane >> 4) * 256);
#pragma unroll
      for (int k = 0; k < 4; ++k) {
        const unsigned xn = ln_apply(xv[k], mb, inv);
        T[(2 * k) * 16 + (lane & 15)] = (u16)xn;
        T[(2 * k + 1) * 16 + (lane & 15)] = (u16)(xn >> 16);
      }
    }
    LGKM0();
    const v4u xt0 = *(LAS v4u*)(L + L_O + rq * 256 + (lane >> 3) * 32), xt1 = *(LAS v4u*)(L + L_O + rq * 256 + (lane >> 3) * 32 + 16);
    STAMP(8);
    float acc[8];
#pragma unroll
    for (int k = 0; k < 8; ++k) acc[k] = 0.f;
#pragma unroll
    for (int i = 0; i < 16; ++i) {
      const unsigned src = (i < 8) ? xt0[(i & 7) >> 1] : xt1[(i & 7) >> 1];
      const unsigned lo = (i & 1) ? (src >> 16) : (src & 0xffffu), hi = (i & 1) ? (src & 0xffff0000u) : (src << 16);
#pragma unroll
      for (int k = 0; k < 4; ++k) { acc[2 * k] = dot2(wv[i][k], lo, acc[2 * k]); acc[2 * k + 1] = dot2(wv[i][k], hi, acc[2 * k + 1]); }
    }
#pragma unroll
    for (int k = 0; k < 8; ++k) { float a = acc[k]; a += dppf<ROR8>(a); acc[k] = x32_sum(x16_sum(a)); }
    const unsigned tag = ep + 1u;
    {
      const int k = (lane >> 3) & 3;
      const float q0 = k == 0 ? acc[0] : k == 1 ? acc[2] : k == 2 ? acc[4] : acc[6];
      const float q1 = k == 0 ? acc[1] : k == 1 ? acc[3] : k == 2 ? acc[5] : acc[7];
      v4u gq = {__float_as_uint(q0), tag, __float_as_uint(q1), tag};
      __builtin_amdgcn_raw_buffer_store_b128(gq, rs, lane < 32 ? WS_QG + head * 4096 + (ch * 4 + rq) * 512 + lane * 16 : OOB, 0, AUX_SC1);
    }
    if (lane == 0) flags[F_TAG] = tag;
    STAMP(1);
    {
      v2u gq[4];
      int off[4];
#pragma unroll
      for (int k = 0; k < 4; ++k) { gq[k] = v2u{0u, 0u}; off[k] = WS_QG + head * 4096 + ((lane >> 5) * 4 + k) * 512 + (lane & 31) * 16; }
      gather<4>(rs, gq, off, tag);
      const float q0 = (__uint_as_float(gq[0].x) + __uint_as_float(gq[1].x)) + (__uint_as_float(gq[2].x) + __uint_as_float(gq[3].x));
      const float q1 = (__uint_as_float(gq[0].y) + __uint_as_float(gq[1].y)) + (__uint_as_float(gq[2].y) + __uint_as_float(gq[3].y));
      *(LAS unsigned*)(L + L_Q + ((lane >> 5) * 32 + 4 * (lane & 7) + ((lane >> 3) & 3)) * 4) = pack2(q0, q1);
      LGKM0();
      if (lane == 0) flags[F_Q] = 1u;
    }
    STAMP(2);
    if (!NUTAB && isM1) {
      if (M1_LATE) lds_wait(flags + F_PUB);
      weight_job(2, L, wo, w1, w2, g, j, m1, lane);
      VM0();
      lds_inc(L, F_W);
      STAMP(4);
    }
    if (isF) {
      const unsigned xb = ((const GAS unsigned*)b2)[4 * m2 + (lane & 3)];
      {
        for (unsigned spin = 0; flags[F_CNT1] < (unsigned)nu && spin < (SPIN_CAP << 4); ++spin) __builtin_amdgcn_s_sleep(4);
        v2u gm[2] = {v2u{0u, 0u}, v2u{0u, 0u}};
        int om[2] = {WS_FL + lane * 16, WS_FL + (64 + lane) * 16};
        gather<2, 4>(rs, gm, om, tag);
        __builtin_amdgcn_s_sleep(F_DELAY);
      }
      STAMP(3);
      v2u gy[3];
      int off[3];
#pragma unroll
      for (int k = 0; k < 3; ++k) { gy[k] = v2u{0u, 0u}; const int pr = 16 * k + (lane >> 2); off[k] = pr < 44 ? WS_P2 + pr * 4096 + (4 * m2 + (lane & 3)) * 16 : OOB; }
      gather<3, 2>(rs, gy, off, tag);
      STAMP(5);
      const bool ish = (lane >> 2) == 11;
      float y0 = __uint_as_float(gy[0].x) + __uint_as_float(gy[1].x) + (ish ? 0.f : __uint_as_float(gy[2].x));
      float y1 = __uint_as_float(gy[0].y) + __uint_as_float(gy[1].y) + (ish ? 0.f : __uint_as_float(gy[2].y));
      float h0 = ish ? __uint_as_float(gy[2].x) : 0.f, h1 = ish ? __uint_as_float(gy[2].y) : 0.f;
      y0 += dppf<ROR4>(y0); y1 += dppf<ROR4>(y1); h0 += dppf<ROR4>(h0); h1 += dppf<ROR4>(h1);
      y0 += dppf<ROR8>(y0); y1 += dppf<ROR8>(y1); h0 += dppf<ROR8>(h0); h1 += dppf<ROR8>(h1);
      y0 = x32_sum(x16_sum(y0)); y1 = x32_sum(x16_sum(y1)); h0 = x32_sum(x16_sum(h0)); h1 = x32_sum(x16_sum(h1));
      const unsigned pr = pack2(h0 + rbf(rbf(y0) + bf2f_lo(xb)), h1 + rbf(rbf(y1) + bf2f_hi(xb)));
      if (lane < 4) ((GAS unsigned*)out)[4 * m2 + lane] = pr;
      if (m2 == 0 && lane == 0) __builtin_amdgcn_raw_buffer_store_b32(tag, rs, WS_EPOCH, 0, AUX_SC1);
      STAMP(6);
    }
  } else {
    const int wv = wave - 1;
    const int rot = b * 5 + (b >> 3) * 3;
    __builtin_amdgcn_s_setprio(2);
    BAR();
    STAMP(7);
#define THROTTLE() do { __builtin_amdgcn_sched_barrier(0); asm volatile("s_waitcnt vmcnt(%0)" :: "n"(WINW - 1)); __builtin_amdgcn_sched_barrier(0); } while (0)
    unsigned tag = 0;
    if (wv >= nu) {
      if (isT1 || isM1) {
        const int job = isT1 ? 0 : (wv == (NUTAB ? 7 : 8)) ? 1 : 2;
        if (isM1 && M1_LATE) lds_wait(flags + F_PUB);
        weight_job(job, L, wo, w1, w2, g, j, m1, lane);
        STAMP(1);
        VM0();
        lds_inc(L, F_W);
        STAMP(4);
        __builtin_amdgcn_s_setprio(2);
        lds_wait(flags + F_Q);
        tag = flags[F_TAG];
      }
    } else {
    const size_t base = ((size_t)head * SK + (SK - WIN) + (u0 + wv) * 64) * (DH * 2);
    const __amdgpu_buffer_rsrc_t rk = __builtin_amdgcn_make_buffer_rsrc((void*)((const char*)kc + base), 0, 16384, 0x00020000);
    const __amdgpu_buffer_rsrc_t rv = __builtin_amdgcn_make_buffer_rsrc((void*)((const char*)vc + base), 0, 16384, 0x00020000);
    v4u K[16], V[16];
#pragma unroll
    for (int i = 0; i < 16; ++i) { if (i >= WINW && i < THRN) THROTTLE();
      K[i] = __builtin_amdgcn_raw_buffer_load_b128(rk, lane * 16, ((i + rot) & 15) * 1024, AUX_NT); }
    __builtin_amdgcn_sched_barrier(0);
    __builtin_amdgcn_s_setprio(0);
    __builtin_amdgcn_sched_barrier(0);
#pragma unroll
    for (int i = 0; i < 16; ++i) V[i] = __builtin_amdgcn_raw_buffer_load_b128(rv, lane * 16, ((i + rot) & 15) * 1024, AUX_NT);
    __builtin_amdgcn_sched_barrier(0);
    __builtin_amdgcn_s_setprio(2);
    STAMP(1);
    lds_wait(flags + F_Q);
    tag = flags[F_TAG];
    const v4u qv = *(LAS v4u*)(L + L_Q + (lane & 15) * 16);
    STAMP(2);
    float d[16];
#pragma unroll
    for (int i = 0; i < 16; ++i) {
      float a = dot2(K[i].x, qv.x, 0.f); a = dot2(K[i].y, qv.y, a); a = dot2(K[i].z, qv.z, a); a = dot2(K[i].w, qv.w, a);
      d[i] = a;
    }
    const int c = lane & 15;
#define TR_STEP(HALF, CTRL, BIT) _Pragma("unroll") for (int hh = 0; hh < (HALF); ++hh) { \
      const float lo_ = d[hh], hi_ = d[hh + (HALF)]; const float t1_ = lo_ + dppf<CTRL>(lo_), t2_ = hi_ + dppf<CTRL>(hi_); d[hh] = (c & (BIT)) ? t2_ : t1_; }
    TR_STEP(8, ROR8, 8) TR_STEP(4, HMIRROR, 4) TR_STEP(2, XOR2, 2) TR_STEP(1, XOR1, 1)
#undef TR_STEP
    const float s = rbf(rbf(d[0]) * 0.08838834764831845f);
    const float mw = wave_max(s);
    if (lane == 0) flags[F_MAX + wv] = __float_as_uint(mw);
    float M;
    {
      unsigned mm;
      for (unsigned spin = 0;; ++spin) {
        mm = flags[F_MAX + (lane & 15)];
        if (all_ok(mm != SENT) || spin > (SPIN_CAP << 4)) break;
        __builtin_amdgcn_s_sleep(1);
      }
      M = __uint_as_float(mm);
      M = fmaxf(M, dppf<ROR8>(M)); M = fmaxf(M, dppf<ROR4>(M)); M = fmaxf(M, dppf<ROR2>(M)); M = fmaxf(M, dppf<ROR1>(M));
    }
    const float p = __builtin_amdgcn_exp2f((s - M) * LOG2E);
    const float l = wave_sum(p);
    if (lane == 0) *(LAS float*)(L + L_ML + wv * 4) = l;
    const unsigned pp = pack2(p, p);
    unsigned mask[4];
#pragma unroll
    for (int t = 0; t < 4; ++t) mask[t] = (c < 8 && (c >> 1) == t) ? ((c & 1) ? 0xffff0000u : 0x0000ffffu) : 0u;
    STAMP(3);
    f32x4 acc = {0.f, 0.f, 0.f, 0.f};
#define PV_STEP(i) { const unsigned pb = __builtin_amdgcn_update_dpp(0u, pp, NEWBCAST + (i), 0xf, 0xf, true); \
      v4u a = {pb & mask[0], pb & mask[1], pb & mask[2], pb & mask[3]}; acc = mfma16(a, V[i], acc); }
    PV_STEP(0) PV_STEP(1) PV_STEP(2) PV_STEP(3) PV_STEP(4) PV_STEP(5) PV_STEP(6) PV_STEP(7)
    PV_STEP(8) PV_STEP(9) PV_STEP(10) PV_STEP(11) PV_STEP(12) PV_STEP(13) PV_STEP(14) PV_STEP(15)
#undef PV_STEP
    if (lane < 32) *(LAS f32x4*)(L + L_O + wv * 512 + (8 * c + 4 * (lane >> 4)) * 4) = acc;
    const int g4 = lane >> 4;
    if (lds_inc(L, F_CNT1) == (unsigned)(nu - 1)) {
      const int dp = 16 * (lane & 3) + 4 * ((lane >> 2) & 3) + (lane >> 4);
      float o0 = 0.f, o1 = 0.f;
#pragma unroll
      for (int u = 0; u < 9; ++u) { const f32x2 ov = *(LAS f32x2*)(L + L_O + (u < nu ? u : 0) * 512 + dp * 8); o0 += u < nu ? ov.x : 0.f; o1 += u < nu ? ov.y : 0.f; }
      float Ls = *(LAS float*)(L + L_ML + (lane & 15) * 4);
      Ls += dppf<ROR8>(Ls); Ls += dppf<ROR4>(Ls); Ls += dppf<ROR2>(Ls); Ls += dppf<ROR1>(Ls);
      v4u go = {__float_as_uint(o0), tag, __float_as_uint(o1), tag};
      __builtin_amdgcn_raw_buffer_store_b128(go, rs, WS_XO + pid * 1024 + lane * 16, 0, AUX_SC1);
      v4u gm = {__float_as_uint(M), tag, __float_as_uint(Ls), tag};
      __builtin_amdgcn_raw_buffer_store_b128(gm, rs, lane == 0 ? WS_XM + pid * 16 : OOB, 0, AUX_SC1);
      CFENCE();
      if (lane == 0) flags[F_PUB] = 1u;
    }
    STAMP(4);
    }
    const int g4 = lane >> 4;
    const f32x4 z4 = {0.f, 0.f, 0.f, 0.f};
    if (isT1 && wv < 8) {
      const int hh = wv >> 2, sh = (wv >> 1) & 1, ch = wv & 1, q = (lane >> 2) & 3, e = lane & 3;
      const int hd = 4 * g + 2 * (j & 1) + hh;
      const int xo = WS_XO + (hd * 8 + 4 * sh + q) * 1024 + (g4 * 4 + e) * 16;
      v2u go[5];
      int off[5];
#pragma unroll
      for (int k = 0; k < 4; ++k) { go[k] = v2u{0u, 0u}; off[k] = xo + k * 256; }
      go[4] = v2u{0u, 0u}; off[4] = WS_XM + (hd * 8 + q + 4 * (g4 & 1)) * 16;
      v4u t[5];
      gather_pass<5>(rs, t, off);
      __builtin_amdgcn_sched_barrier(0);
      for (unsigned spin = 0; flags[F_W] < 1u && spin < (SPIN_CAP << 4); ++spin) __builtin_amdgcn_s_sleep(1);
      CFENCE();
      v4u Bf[4][2];
#pragma unroll
      for (int ee = 0; ee < 4; ++ee)
#pragma unroll
        for (int cb = 0; cb < 2; ++cb) {
          LAS unsigned char* a0 = L + L_W + (16 * hh + 4 * ee + g4) * 1024 + ch * 512 + ((lane & 15) >> 2) * 64 + (16 * cb + 4 * (lane & 3)) * 2;
          const v2u lo = tr_read(a0), hi = tr_read(a0 + 4 * 64);
          Bf[ee][cb] = v4u{lo.x, lo.y, hi.x, hi.y};
        }
      __builtin_amdgcn_sched_barrier(0);
      const bool ok0 = gather_check<5>(t, go, off, tag);
      STAMP(9);
      if (!ok0) gather<5, T1_SLEEP>(rs, go, off, tag);
      STAMP(5);
      const float mm = __uint_as_float(go[4].x), ll = __uint_as_float(go[4].y);
      float Mx = fmaxf(mm, dppf<ROR4>(mm)); Mx = fmaxf(Mx, dppf<ROR8>(Mx)); Mx = x16_max(Mx);
      const float f = __builtin_amdgcn_exp2f((mm - Mx) * LOG2E);
      float Ls = f * ll; Ls += dppf<ROR4>(Ls); Ls += dppf<ROR8>(Ls); Ls = x16_sum(Ls);
      const float fn = f * rcp(Ls);
      const auto sw = __builtin_amdgcn_permlane16_swap(__float_as_uint(fn), __float_as_uint(fn), false, false);
      const float fo = __uint_as_float(sh ? sw[1] : sw[0]);
      v4u A;
#pragma unroll
      for (int k = 0; k < 4; ++k) A[k] = pack2(fo * __uint_as_float(go[k].x), fo * __uint_as_float(go[k].y));
      float y[2];
#pragma unroll
      for (int cb = 0; cb < 2; ++cb) {
        const f32x4 D0 = mfma16(A, Bf[0][cb], z4), D1 = mfma16(A, Bf[1][cb], z4), D2 = mfma16(A, Bf[2][cb], z4), D3 = mfma16(A, Bf[3][cb], z4);
        y[cb] = (D0[0] + D1[1]) + (D2[2] + D3[3]);
      }
      const float y0 = x32_sum(x16_sum(y[0])), y1 = x32_sum(x16_sum(y[1]));
      if (lane < 32) *(LAS float*)(L + L_RED + wv * 128 + lane * 4) = (lane < 16) ? y0 : y1;
      BAR();
      if (wv == 0) {
        const int cc = lane & 31;
        float z0 = 0.f, z1 = 0.f;
#pragma unroll
        for (int u = 0; u < 4; ++u) { const f32x2 r = *(LAS f32x2*)(L + L_RED + (2 * u + (cc >> 4)) * 128 + (cc & 15) * 8); z0 += r.x; z1 += r.y; }
        v4u gy = {__float_as_uint(z0), tag, __float_as_uint(z1), tag};
        __builtin_amdgcn_raw_buffer_store_b128(gy, rs, lane < 32 ? WS_GG + (2 * g + (j & 1)) * 4096 + (32 * (j >> 1) + lane) * 16 : lane == 32 ? WS_FL + (g * 16 + j) * 16 : OOB, 0, AUX_SC1);
      }
      STAMP(6);
    } else if (isM1 && wv < 8) {
      const int half = (lane >> 3) & 1, tt = (lane >> 2) & 1, e = lane & 3;
      const int pj = 32 * wv + 16 * tt + 4 * g4 + e;
      const unsigned xw = ((const GAS unsigned*)x)[pj];
      const unsigned bias = b1[32 * m1 + 8 * g4 + (lane & 7)];
      for (unsigned spin = 0; flags[F_W] < 2u && spin < (SPIN_CAP << 4); ++spin) __builtin_amdgcn_s_sleep(1);
      CFENCE();
      v4u B1[2][2], B2[4];
#pragma unroll
      for (int t2 = 0; t2 < 2; ++t2)
#pragma unroll
        for (int cb = 0; cb < 2; ++cb) {
          LAS unsigned char* a0 = L + L_W + (64 * wv + 32 * t2 + 8 * g4 + ((lane & 15) >> 2)) * 64 + (16 * cb + 4 * (lane & 3)) * 2;
          const v2u lo = tr_read(a0), hi = tr_read(a0 + 4 * 64);
          B1[t2][cb] = v4u{lo.x, lo.y, hi.x, hi.y};
        }
#pragma unroll
      for (int cb = 0; cb < 4; ++cb) {
        LAS unsigned char* a0 = L + L_W + 32768 + wv * 4096 + g4 * 1024 + (cb >> 1) * 512 + ((lane & 15) >> 2) * 64 + (16 * (cb & 1) + 4 * (lane & 3)) * 2;
        const v2u lo = tr_read(a0), hi = tr_read(a0 + 4 * 64);
        B2[cb] = v4u{lo.x, lo.y, hi.x, hi.y};
      }
      STAMP(8);
      v2u gy[8];
      int off[8];
#pragma unroll
      for (int k = 0; k < 8; ++k) { gy[k] = v2u{0u, 0u}; off[k] = WS_GG + (8 * half + k) * 4096 + pj * 16; }
      gather<8, 2>(rs, gy, off, tag);
      STAMP(5);
      float y0 = ((__uint_as_float(gy[0].x) + __uint_as_float(gy[1].x)) + (__uint_as_float(gy[2].x) + __uint_as_float(gy[3].x))) + ((__uint_as_float(gy[4].x) + __uint_as_float(gy[5].x)) + (__uint_as_float(gy[6].x) + __uint_as_float(gy[7].x)));
      float y1 = ((__uint_as_float(gy[0].y) + __uint_as_float(gy[1].y)) + (__uint_as_float(gy[2].y) + __uint_as_float(gy[3].y))) + ((__uint_as_float(gy[4].y) + __uint_as_float(gy[5].y)) + (__uint_as_float(gy[6].y) + __uint_as_float(gy[7].y)));
      y0 += dppf<ROR8>(y0); y1 += dppf<ROR8>(y1);
      const unsigned hp = pack2(bf2f_lo(xw) + rbf(y0), bf2f_hi(xw) + rbf(y1));
      const float h0 = bf2f_lo(hp), h1 = bf2f_hi(hp);
      if (m1 == 0) {
        v4u gh = {__float_as_uint(h0), tag, __float_as_uint(h1), tag};
        __builtin_amdgcn_raw_buffer_store_b128(gh, rs, half == 0 ? WS_P2 + 43 * 4096 + pj * 16 : OOB, 0, AUX_SC1);
      }
      {
        const float cp = __builtin_bit_cast(float, __builtin_amdgcn_readfirstlane(__builtin_bit_cast(int, h0)));
        const float d0 = h0 - cp, d1 = h1 - cp;
        float s1 = d0 + d1, s2 = fmaf(d0, d0, d1 * d1);
        s1 += dppf<XOR1>(s1); s2 += dppf<XOR1>(s2); s1 += dppf<XOR2>(s1); s2 += dppf<XOR2>(s2);
        s1 += dppf<HMIRROR>(s1); s2 += dppf<HMIRROR>(s2);
        s1 = x16_sum(s1); s2 = x16_sum(s2); s1 = x32_sum(s1); s2 = x32_sum(s2);
        const float mw = fmaf(s1, 1.f / 64.f, cp), m2w = fmaf(-s1 * (1.f / 64.f), s1, s2);
        if (lane == 0) *(LAS f32x2*)(L + L_ST + wv * 8) = f32x2{mw, m2w};
      }
      BAR();
      STAMP(9);
      float mb, inv;
      {
        const f32x2 st = *(LAS f32x2*)(L + L_ST + (lane & 7) * 8);
        const float mean = sum8(st.x) * 0.125f;
        const float dm = st.x - mean;
        const float var = rbf(fmaxf(sum8(fmaf(64.f * dm, dm, st.y)) * (1.f / 512.f), 0.f));
        mb = rbf(mean);
        inv = rcp(rbf(__builtin_amdgcn_sqrtf(rbf(var + 1e-5f))));
      }
      const unsigned hn = ln_apply(hp, mb, inv);
      const v4u A = {dppu<0x00>(hn), dppu<0x55>(hn), dppu<0xAA>(hn), dppu<0xFF>(hn)};
      const f32x4 D00 = mfma16(A, B1[0][0], z4), D10 = mfma16(A, B1[1][0], z4), D01 = mfma16(A, B1[0][1], z4), D11 = mfma16(A, B1[1][1], z4);
      const float r0 = x16_sum(g4 == 0 ? D00[0] : D10[0]), r1 = x16_sum(g4 == 0 ? D01[0] : D11[0]);
      if (lane < 16) { *(LAS float*)(L + L_RED + wv * 128 + lane * 4) = r0; *(LAS float*)(L + L_RED + wv * 128 + 64 + lane * 4) = r1; }
      BAR();
      float a = 0.f;
#pragma unroll
      for (int u = 0; u < 8; ++u) a += *(LAS float*)(L + L_RED + u * 128 + (8 * g4 + (lane & 7)) * 4);
      a = rbf(rbf(a) + bf2f_lo(bias));
      const float tq = 0.7978845608028654f * (a + 0.044715f * a * a * a);
      const float ex = __builtin_amdgcn_exp2f(tq * (2.f * LOG2E));
      const float th = 1.f - 2.f * rcp(ex + 1.f);
      const float ge = 0.5f * a * (1.f + th);
      const unsigned pr = pack2(ge, dppf<XOR1>(ge));
      const v4u A2 = {(unsigned)__builtin_amdgcn_update_dpp(0u, pr, NEWBCAST + 0, 0xf, 0xf, true), (unsigned)__builtin_amdgcn_update_dpp(0u, pr, NEWBCAST + 2, 0xf, 0xf, true),
                      (unsigned)__builtin_amdgcn_update_dpp(0u, pr, NEWBCAST + 4, 0xf, 0xf, true), (unsigned)__builtin_amdgcn_update_dpp(0u, pr, NEWBCAST + 6, 0xf, 0xf, true)};
      const f32x4 E0 = mfma16(A2, B2[0], z4), E1 = mfma16(A2, B2[1], z4), E2 = mfma16(A2, B2[2], z4), E3 = mfma16(A2, B2[3], z4);
      const float v0 = g4 == 0 ? E0[0] : g4 == 1 ? E1[0] : g4 == 2 ? E2[0] : E3[0];
      const float v1 = dppf<XOR1>(v0);
      v4u gp = {__float_as_uint(v0), tag, __float_as_uint(v1), tag};
      __builtin_amdgcn_raw_buffer_store_b128(gp, rs, (lane & 1) == 0 ? WS_P2 + m1 * 4096 + (32 * wv + 8 * g4 + ((lane & 15) >> 1)) * 16 : OOB, 0, AUX_SC1);
      STAMP(6);
    }
  }
  STAMP_FLUSH();
}
}

extern "C" void kernel_launch(void* const* d_in, const int* in_sizes, int n_in, void* d_out, int out_size, void* d_ws, size_t ws_size, hipStream_t stream) {
  (void)in_sizes; (void)n_in; (void)out_size; (void)ws_size;
  (void)hipFuncSetAttribute((const void*)fused_decode, hipFuncAttributeMaxDynamicSharedMemorySize, LDS_BYTES);
  hipLaunchKernelGGL(fused_decode, dim3(NWG), dim3(NTHR), LDS_BYTES, stream,
                     (const u16*)d_in[0], (const u16*)d_in[1], (const u16*)d_in[2], (const u16*)d_in[3], (const u16*)d_in[4],
                     (const u16*)d_in[6], (const u16*)d_in[7], (const u16*)d_in[8], (const u16*)d_in[9], (u16*)d_out, (unsigned char*)d_ws);
}
```

```cpp
#include <hip/hip_runtime.h>
#include <stdint.h>

typedef unsigned v4u __attribute__((ext_vector_type(4)));
typedef unsigned v2u __attribute__((ext_vector_type(2)));
typedef float f32x4 __attribute__((ext_vector_type(4)));
typedef float f32x2 __attribute__((ext_vector_type(2)));
typedef short v4s __attribute__((ext_vector_type(4)));
typedef __bf16 bf16x2 __attribute__((ext_vector_type(2)));
typedef __bf16 bf16x8 __attribute__((ext_vector_type(8)));
typedef unsigned short u16;
#define LAS __attribute__((address_space(3)))
#define GAS __attribute__((address_space(1)))

#define WQ_SEG 32
#define WINW 4
#define VSPLIT 2
#define ISSN 8
#define T1_WAIT 0
#define M1_WAIT 1
#define THRN 12
#define T1_SLEEP 4
#define M1_GATE 0
#define M1_DELAY 12
#define F_GATE 1
#define F_DELAY 4
#define DPRIO0 2
#define DPRIO1 0
#define MPRIO0 0
#define MPRIO1 0
#define NUTAB 1
#define M1_LATE 1
#define KAUX 2
#define VAUX 2
#define DWIN 4
#define DTHRN 12
#define W1_AUX 2
#define WQ_AUX 2
namespace {
constexpr int SK = 16384, DH = 128, WIN = 4096, FF = 1376;
#define NWAVES 10
constexpr int NWG = 256, NTHR = 64 * NWAVES;
constexpr int WS_EPOCH = 0;
constexpr int WS_XM = 4096;
constexpr int WS_FL = 8192;
constexpr int WS_QG = 12288;
constexpr int WS_XO = WS_QG + 131072;
constexpr int WS_GG = WS_XO + 262144;
constexpr int WS_P2 = WS_GG + 65536;
constexpr int WS_END = WS_P2 + 44 * 4096;
constexpr int WS_STAMP = 1 << 20;
static_assert(WS_END <= WS_STAMP, "ws");
constexpr int L_W = 0;
constexpr int L_Q = 65536;
constexpr int L_FLAG = L_Q + 256;
constexpr int L_ML = L_FLAG + 128;
constexpr int L_O = L_ML + 64;
constexpr int L_RED = L_O + 4608;
constexpr int L_ST = L_RED + 1024;
constexpr int L_END = L_ST + 128;
constexpr int LDS_BYTES = 84 * 1024;
static_assert(L_END <= LDS_BYTES, "lds");
constexpr int F_Q = 0, F_TAG = 1, F_CNT1 = 2, F_W = 4, F_GO = 5, F_PUB = 6, F_MAX = 8;
constexpr unsigned SENT = 0x7fffffffu;
constexpr int OOB = 0x7ffffff0;
constexpr int AUX_NT = 2, AUX_SC1 = 16;
constexpr unsigned SPIN_CAP = 1u << 16;
constexpr float LOG2E = 1.4426950408889634f;

__device__ __forceinline__ float bf2f_lo(unsigned u) { return __builtin_bit_cast(float, u << 16); }
__device__ __forceinline__ float bf2f_hi(unsigned u) { return __builtin_bit_cast(float, u & 0xffff0000u); }
__device__ __forceinline__ unsigned pack2(float a, float b) { bf16x2 r = {(__bf16)a, (__bf16)b}; return __builtin_bit_cast(unsigned, r); }
__device__ __forceinline__ float rbf(float f) { return bf2f_lo(pack2(f, f)); }
__device__ __forceinline__ float dot2(unsigned a, unsigned b, float acc) {
  return __builtin_amdgcn_fdot2_f32_bf16(__builtin_bit_cast(bf16x2, a), __builtin_bit_cast(bf16x2, b), acc, false);
}
template <int CTRL> __device__ __forceinline__ unsigned dppu(unsigned x) { return __builtin_amdgcn_mov_dpp(x, CTRL, 0xf, 0xf, true); }
template <int CTRL> __device__ __forceinline__ float dppf(float x) { return __builtin_bit_cast(float, dppu<CTRL>(__builtin_bit_cast(unsigned, x))); }
constexpr int XOR1 = 0xB1, XOR2 = 0x4E, HMIRROR = 0x141, ROR1 = 0x121, ROR2 = 0x122, ROR4 = 0x124, ROR8 = 0x128, NEWBCAST = 0x150;
__device__ __forceinline__ float x16_sum(float x) { auto s = __builtin_amdgcn_permlane16_swap(__float_as_uint(x), __float_as_uint(x), false, false); return __uint_as_float(s[0]) + __uint_as_float(s[1]); }
__device__ __forceinline__ float x32_sum(float x) { auto s = __builtin_amdgcn_permlane32_swap(__float_as_uint(x), __float_as_uint(x), false, false); return __uint_as_float(s[0]) + __uint_as_float(s[1]); }
__device__ __forceinline__ float x16_max(float x) { auto s = __builtin_amdgcn_permlane16_swap(__float_as_uint(x), __float_as_uint(x), false, false); return fmaxf(__uint_as_float(s[0]), __uint_as_float(s[1])); }
__device__ __forceinline__ float x32_max(float x) { auto s = __builtin_amdgcn_permlane32_swap(__float_as_uint(x), __float_as_uint(x), false, false); return fmaxf(__uint_as_float(s[0]), __uint_as_float(s[1])); }
__device__ __forceinline__ float wave_sum(float x) {
  x += dppf<ROR8>(x); x += dppf<ROR4>(x); x += dppf<ROR2>(x); x += dppf<ROR1>(x);
  return x32_sum(x16_sum(x));
}
__device__ __forceinline__ float wave_max(float x) {
  x = fmaxf(x, dppf<ROR8>(x)); x = fmaxf(x, dppf<ROR4>(x)); x = fmaxf(x, dppf<ROR2>(x)); x = fmaxf(x, dppf<ROR1>(x));
  return x32_max(x16_max(x));
}
__device__ __forceinline__ f32x4 mfma16(v4u a, v4u b, f32x4 c) {
  return __builtin_amdgcn_mfma_f32_16x16x32_bf16(__builtin_bit_cast(bf16x8, a), __builtin_bit_cast(bf16x8, b), c, 0, 0, 0);
}
__device__ __forceinline__ v2u tr_read(LAS unsigned char* p) { return __builtin_bit_cast(v2u, __builtin_amdgcn_ds_read_tr16_b64_v4i16((LAS v4s*)p)); }
#define LGKM0() asm volatile("s_waitcnt lgkmcnt(0)" ::: "memory")
#define VM0() asm volatile("s_waitcnt vmcnt(0)" ::: "memory")
#define BAR() do { asm volatile("s_waitcnt lgkmcnt(0)" ::: "memory"); __builtin_amdgcn_s_barrier(); asm volatile("" ::: "memory"); } while (0)
#define CFENCE() asm volatile("" ::: "memory")
__device__ __forceinline__ bool all_ok(bool ok) { return __builtin_amdgcn_ballot_w64(!ok) == 0ull; }

__device__ __forceinline__ void ln_stats(const float (&v)[8], float& mb, float& inv) {
  const float c = __builtin_bit_cast(float, __builtin_amdgcn_readfirstlane(__builtin_bit_cast(int, v[0])));
  float s = 0.f, q = 0.f;
#pragma unroll
  for (int j = 0; j < 8; ++j) { const float d = v[j] - c; s += d; q = fmaf(d, d, q); }
  s += dppf<ROR8>(s); q += dppf<ROR8>(q); s += dppf<ROR4>(s); q += dppf<ROR4>(q);
  s += dppf<ROR2>(s); q += dppf<ROR2>(q); s += dppf<ROR1>(s); q += dppf<ROR1>(q);
  s = x16_sum(s); q = x16_sum(q); s = x32_sum(s); q = x32_sum(q);
  const float ms = s * (1.f / 512.f);
  const float var = rbf(fmaxf(fmaf(-ms, ms, q * (1.f / 512.f)), 0.f));
  mb = rbf(c + ms);
  const float den = rbf(__builtin_amdgcn_sqrtf(rbf(var + 1e-5f)));
  inv = __builtin_amdgcn_rcpf(den);
}
__device__ __forceinline__ float rcp(float x) { return __builtin_amdgcn_rcpf(x); }
__device__ __forceinline__ unsigned ln_apply(unsigned pr, float mb, float inv) { return pack2(rbf(bf2f_lo(pr) - mb) * inv, rbf(bf2f_hi(pr) - mb) * inv); }
__device__ __forceinline__ float sum8(float x) { x += dppf<XOR1>(x); x += dppf<XOR2>(x); x += dppf<HMIRROR>(x); return x; }
__device__ __forceinline__ float max8(float x) { x = fmaxf(x, dppf<XOR1>(x)); x = fmaxf(x, dppf<XOR2>(x)); x = fmaxf(x, dppf<HMIRROR>(x)); return x; }
template <int N> __device__ __forceinline__ void gather_pass(__amdgpu_buffer_rsrc_t rs, v4u (&t)[N], const int (&off)[N]) {
  CFENCE();
#pragma unroll
  for (int k = 0; k < N; ++k) t[k] = __builtin_amdgcn_raw_buffer_load_b128(rs, off[k], 0, AUX_SC1);
}
template <int N> __device__ __forceinline__ bool gather_check(const v4u (&t)[N], v2u (&g)[N], int (&off)[N], unsigned tag) {
  bool all = true;
#pragma unroll
  for (int k = 0; k < N; ++k) {
    const bool live = off[k] != OOB;
    const bool ok = live && t[k].y == tag && t[k].w == tag;
    g[k].x = ok ? t[k].x : g[k].x; g[k].y = ok ? t[k].z : g[k].y;
    off[k] = ok ? OOB : off[k];
    all = all && (ok || !live);
  }
  return all_ok(all);
}
template <int N, int SLEEP = 0> __device__ __forceinline__ void gather(__amdgpu_buffer_rsrc_t rs, v2u (&g)[N], int (&off)[N], unsigned tag) {
  for (unsigned spin = 0;; ++spin) {
    v4u t[N];
    gather_pass<N>(rs, t, off);
    if (gather_check<N>(t, g, off, tag) || spin > SPIN_CAP) break;
    if (SLEEP) __builtin_amdgcn_s_sleep(SLEEP);
  }
}
__device__ __forceinline__ void lds_wait(volatile LAS unsigned* f) {
  for (unsigned spin = 0; *f == 0u && spin < (SPIN_CAP << 4); ++spin) __builtin_amdgcn_s_sleep(1);
  CFENCE();
}
__device__ __forceinline__ unsigned lds_inc(LAS unsigned char* L, int w) {
  unsigned r = 0;
  asm volatile("" ::: "memory");
  if ((threadIdx.x & 63) == 0) r = __hip_atomic_fetch_add((LAS unsigned*)(L + L_FLAG + w * 4), 1u, __ATOMIC_RELAXED, __HIP_MEMORY_SCOPE_WORKGROUP);
  r = __builtin_amdgcn_readfirstlane(r);
  asm volatile("" ::: "memory");
  return r;
}

template <bool NT> __device__ __forceinline__ void dma16(__amdgpu_buffer_rsrc_t rs, int voff, int soff, unsigned lds) {
  if (NT) asm volatile("s_mov_b32 m0, %0\n\ts_nop 0\n\tbuffer_load_dwordx4 %1, %2, %3 offen nt lds" :: "s"(lds), "v"(voff), "s"(rs), "s"(soff) : "m0", "memory");
  else asm volatile("s_mov_b32 m0, %0\n\ts_nop 0\n\tbuffer_load_dwordx4 %1, %2, %3 offen lds" :: "s"(lds), "v"(voff), "s"(rs), "s"(soff) : "m0", "memory");
}

__device__ __forceinline__ void weight_job(int job, LAS unsigned char* L, const u16* wo, const u16* w1, const u16* w2, int g, int j, int m1, int lane) {
  const bool isT1 = job == 0, isw1 = job == 1;
  const int tl = ((lane >> 2) & 7), tb = (lane >> 5) * 64 + (lane & 3) * 16;
  const void* wp = isT1 ? (const void*)wo : isw1 ? (const void*)w1 : (const void*)w2;
  const __amdgpu_buffer_rsrc_t rw = __builtin_amdgcn_make_buffer_rsrc((void*)wp, 0, isT1 ? 4096 * 1024 : FF * 1024, 0x00020000);
  const int vo = isT1 ? ((4 * g + 2 * (j & 1)) * 128 + tl) * 1024 + 128 * (j >> 1) + tb
               : isw1 ? (lane >> 2) * (FF * 2) + 64 * m1 + (lane & 3) * 16 : (32 * m1 + tl) * 1024 + tb;
  const int s1 = isT1 ? 8192 : isw1 ? 16 * FF * 2 : 8192, s4 = isT1 ? 4 * 8192 : isw1 ? 4 * 16 * FF * 2 : 128;
  const unsigned lb = (unsigned)(uintptr_t)(L + L_W) + (job == 2 ? 32768 : 0);
#pragma unroll
  for (int i = 0; i < 32; ++i) {
    if (i == 0) { if (isT1) __builtin_amdgcn_s_setprio(DPRIO0); else __builtin_amdgcn_s_setprio(MPRIO0); }
    if (i >= DWIN && i < DTHRN && (isT1 || !M1_LATE)) { __builtin_amdgcn_sched_barrier(0); asm volatile("s_waitcnt vmcnt(%0)" :: "n"(DWIN - 1)); __builtin_amdgcn_sched_barrier(0); }
    if (i == 16) { if (isT1) __builtin_amdgcn_s_setprio(DPRIO1); else __builtin_amdgcn_s_setprio(MPRIO1); }
    dma16<true>(rw, vo, (i & 3) * s1 + (i >> 2) * s4, lb + i * 1024);
  }
}

#define STAMP_DECL
#define STAMP(i) do {} while (0)
#define STAMP_FLUSH() do {} while (0)

__global__ void __launch_bounds__(NTHR, 3)
fused_decode(const u16* __restrict__ x, const u16* __restrict__ kc, const u16* __restrict__ vc, const u16* __restrict__ wq, const u16* __restrict__ wo,
             const u16* __restrict__ w1, const u16* __restrict__ b1, const u16* __restrict__ w2, const u16* __restrict__ b2, u16* __restrict__ out, unsigned char* ws) {
  extern __shared__ __attribute__((aligned(16))) unsigned char smem[];
  LAS unsigned char* L = (LAS unsigned char*)smem;
  const int tid = threadIdx.x, lane = tid & 63, wave = __builtin_amdgcn_readfirstlane(tid >> 6);
  const int b = blockIdx.x, g = b & 7, j = b >> 3;
  const int head = 4 * g + (j & 3), sp = j >> 2;
  const int pid = head * 8 + sp;
  const bool rlo = (j & 3) < 2;
  const int nu = sp < 4 ? 8 : sp == 4 ? 7 : sp == 5 ? (rlo ? 7 : 8) : sp == 6 ? (rlo ? 9 : 8) : 9;
  const int u0 = sp <= 4 ? 8 * sp : sp == 5 ? 39 : sp == 6 ? (rlo ? 46 : 47) : 55;
  const int jr = j - 16;
  const bool isT1 = j < 16;
  const int m1 = g * 6 + jr;
  const bool isM1 = (jr >= 0) && (jr < 6) && (m1 < 43);
  const int m2 = g * 8 + (jr - 6);
  const bool isF = (jr >= 6) && (jr < 14);
  const __amdgpu_buffer_rsrc_t rs = __builtin_amdgcn_make_buffer_rsrc((void*)ws, 0, WS_END, 0x00020000);
  volatile LAS unsigned* flags = (volatile LAS unsigned*)(L + L_FLAG);
  STAMP_DECL
  STAMP(0);

  if (wave == 0) {
    __builtin_amdgcn_s_setprio(3);
    const __amdgpu_buffer_rsrc_t rx = __builtin_amdgcn_make_buffer_rsrc((void*)x, 0, 1024, 0x00020000);
    const v4u xv = __builtin_amdgcn_raw_buffer_load_b128(rx, lane * 16, 0, 0);
    __builtin_amdgcn_sched_barrier(0);
    v4u wv[16];
    const int rq = sp >> 1, ch = sp & 1;
    {
      const __amdgpu_buffer_rsrc_t rw = __builtin_amdgcn_make_buffer_rsrc((void*)wq, 0, 512 * 8192, 0x00020000);
      const int vo = (rq * 128 + (lane >> 3)) * 8192 + (head * 128 + 64 * ch + 8 * (lane & 7)) * 2;
#pragma unroll
      for (int i = 0; i < 16; ++i) wv[i] = __builtin_amdgcn_raw_buffer_load_b128(rw, vo, i * 8 * 8192, AUX_NT);
    }
    if (lane < 48) flags[lane] = (lane >= F_MAX && lane < F_MAX + 16) ? (lane < F_MAX + nu ? SENT : 0xff800000u) : 0u;
    __builtin_amdgcn_sched_barrier(0);
    BAR();
    __builtin_amdgcn_sched_barrier(0);
    const unsigned ep = *(const __attribute__((address_space(4))) unsigned*)(ws + WS_EPOCH);
    STAMP(7);
    float xf[8];
#pragma unroll
    for (int k = 0; k < 4; ++k) { xf[2 * k] = bf2f_lo(xv[k]); xf[2 * k + 1] = bf2f_hi(xv[k]); }
    float mb, inv;
    ln_stats(xf, mb, inv);
    {
      LAS u16* T = (LAS u16*)(L + L_O + (lane >> 4) * 256);
#pragma unroll
      for (int k = 0; k < 4; ++k) {
        const unsigned xn = ln_apply(xv[k], mb, inv);
        T[(2 * k) * 16 + (lane & 15)] = (u16)xn;
        T[(2 * k + 1) * 16 + (lane & 15)] = (u16)(xn >> 16);
      }
    }
    LGKM0();
    const v4u xt0 = *(LAS v4u*)(L + L_O + rq * 256 + (lane >> 3) * 32), xt1 = *(LAS v4u*)(L + L_O + rq * 256 + (lane >> 3) * 32 + 16);
    STAMP(8);
    float acc[8];
#pragma unroll
    for (int k = 0; k < 8; ++k) acc[k] = 0.f;
#pragma unroll
    for (int i = 0; i < 16; ++i) {
      const unsigned src = (i < 8) ? xt0[(i & 7) >> 1] : xt1[(i & 7) >> 1];
      const unsigned lo = (i & 1) ? (src >> 16) : (src & 0xffffu), hi = (i & 1) ? (src & 0xffff0000u) : (src << 16);
#pragma unroll
      for (int k = 0; k < 4; ++k) { acc[2 * k] = dot2(wv[i][k], lo, acc[2 * k]); acc[2 * k + 1] = dot2(wv[i][k], hi, acc[2 * k + 1]); }
    }
#pragma unroll
    for (int k = 0; k < 8; ++k) { float a = acc[k]; a += dppf<ROR8>(a); acc[k] = x32_sum(x16_sum(a)); }
    const unsigned tag = ep + 1u;
    {
      const int k = (lane >> 3) & 3;
      const float q0 = k == 0 ? acc[0] : k == 1 ? acc[2] : k == 2 ? acc[4] : acc[6];
      const float q1 = k == 0 ? acc[1] : k == 1 ? acc[3] : k == 2 ? acc[5] : acc[7];
      v4u gq = {__float_as_uint(q0), tag, __float_as_uint(q1), tag};
      __builtin_amdgcn_raw_buffer_store_b128(gq, rs, lane < 32 ? WS_QG + head * 4096 + (ch * 4 + rq) * 512 + lane * 16 : OOB, 0, AUX_SC1);
    }
    if (lane == 0) flags[F_TAG] = tag;
    STAMP(1);
    {
      v2u gq[4];
      int off[4];
#pragma unroll
      for (int k = 0; k < 4; ++k) { gq[k] = v2u{0u, 0u}; off[k] = WS_QG + head * 4096 + ((lane >> 5) * 4 + k) * 512 + (lane & 31) * 16; }
      gather<4>(rs, gq, off, tag);
      const float q0 = (__uint_as_float(gq[0].x) + __uint_as_float(gq[1].x)) + (__uint_as_float(gq[2].x) + __uint_as_float(gq[3].x));
      const float q1 = (__uint_as_float(gq[0].y) + __uint_as_float(gq[1].y)) + (__uint_as_float(gq[2].y) + __uint_as_float(gq[3].y));
      *(LAS unsigned*)(L + L_Q + ((lane >> 5) * 32 + 4 * (lane & 7) + ((lane >> 3) & 3)) * 4) = pack2(q0, q1);
      LGKM0();
      if (lane == 0) flags[F_Q] = 1u;
    }
    STAMP(2);
    if (!NUTAB && isM1) {
      if (M1_LATE) lds_wait(flags + F_PUB);
      weight_job(2, L, wo, w1, w2, g, j, m1, lane);
      VM0();
      lds_inc(L, F_W);
      STAMP(4);
    }
    if (isF) {
      const unsigned xb = ((const GAS unsigned*)b2)[4 * m2 + (lane & 3)];
      {
        for (unsigned spin = 0; flags[F_CNT1] < (unsigned)nu && spin < (SPIN_CAP << 4); ++spin) __builtin_amdgcn_s_sleep(4);
        v2u gm[2] = {v2u{0u, 0u}, v2u{0u, 0u}};
        int om[2] = {WS_FL + lane * 16, WS_FL + (64 + lane) * 16};
        gather<2, 4>(rs, gm, om, tag);
        __builtin_amdgcn_s_sleep(F_DELAY);
      }
      STAMP(3);
      v2u gy[3];
      int off[3];
#pragma unroll
      for (int k = 0; k < 3; ++k) { gy[k] = v2u{0u, 0u}; const int pr = 16 * k + (lane >> 2); off[k] = pr < 44 ? WS_P2 + pr * 4096 + (4 * m2 + (lane & 3)) * 16 : OOB; }
      gather<3, 2>(rs, gy, off, tag);
      STAMP(5);
      const bool ish = (lane >> 2) == 11;
      float y0 = __uint_as_float(gy[0].x) + __uint_as_float(gy[1].x) + (ish ? 0.f : __uint_as_float(gy[2].x));
      float y1 = __uint_as_float(gy[0].y) + __uint_as_float(gy[1].y) + (ish ? 0.f : __uint_as_float(gy[2].y));
      float h0 = ish ? __uint_as_float(gy[2].x) : 0.f, h1 = ish ? __uint_as_float(gy[2].y) : 0.f;
      y0 += dppf<ROR4>(y0); y1 += dppf<ROR4>(y1); h0 += dppf<ROR4>(h0); h1 += dppf<ROR4>(h1);
      y0 += dppf<ROR8>(y0); y1 += dppf<ROR8>(y1); h0 += dppf<ROR8>(h0); h1 += dppf<ROR8>(h1);
      y0 = x32_sum(x16_sum(y0)); y1 = x32_sum(x16_sum(y1)); h0 = x32_sum(x16_sum(h0)); h1 = x32_sum(x16_sum(h1));
      const unsigned pr = pack2(h0 + rbf(rbf(y0) + bf2f_lo(xb)), h1 + rbf(rbf(y1) + bf2f_hi(xb)));
      if (lane < 4) ((GAS unsigned*)out)[4 * m2 + lane] = pr;
      if (m2 == 0 && lane == 0) __builtin_amdgcn_raw_buffer_store_b32(tag, rs, WS_EPOCH, 0, AUX_SC1);
      STAMP(6);
    }
  } else {
    const int wv = wave - 1;
    const int rot = b * 5 + (b >> 3) * 3;
    __builtin_amdgcn_s_setprio(2);
    BAR();
    STAMP(7);
#define THROTTLE() do { __builtin_amdgcn_sched_barrier(0); asm volatile("s_waitcnt vmcnt(%0)" :: "n"(WINW - 1)); __builtin_amdgcn_sched_barrier(0); } while (0)
    unsigned tag = 0;
    if (wv >= nu) {
      if (isT1 || isM1) {
        const int job = isT1 ? 0 : (wv == (NUTAB ? 7 : 8)) ? 1 : 2;
        if (isM1 && M1_LATE) lds_wait(flags + F_PUB);
        weight_job(job, L, wo, w1, w2, g, j, m1, lane);
        STAMP(1);
        VM0();
        lds_inc(L, F_W);
        STAMP(4);
        __builtin_amdgcn_s_setprio(2);
        lds_wait(flags + F_Q);
        tag = flags[F_TAG];
      }
    } else {
    const size_t base = ((size_t)head * SK + (SK - WIN) + (u0 + wv) * 64) * (DH * 2);
    const __amdgpu_buffer_rsrc_t rk = __builtin_amdgcn_make_buffer_rsrc((void*)((const char*)kc + base), 0, 16384, 0x00020000);
    const __amdgpu_buffer_rsrc_t rv = __builtin_amdgcn_make_buffer_rsrc((void*)((const char*)vc + base), 0, 16384, 0x00020000);
    v4u K[16], V[16];
#pragma unroll
    for (int i = 0; i < 16; ++i) { if (i >= WINW && i < THRN) THROTTLE();
      K[i] = __builtin_amdgcn_raw_buffer_load_b128(rk, lane * 16, ((i + rot) & 15) * 1024, KAUX); }
    __builtin_amdgcn_sched_barrier(0);
    __builtin_amdgcn_s_setprio(0);
    __builtin_amdgcn_sched_barrier(0);
#pragma unroll
    for (int i = 0; i < 16; ++i) V[i] = __builtin_amdgcn_raw_buffer_load_b128(rv, lane * 16, ((i + rot) & 15) * 1024, VAUX);
    __builtin_amdgcn_sched_barrier(0);
    __builtin_amdgcn_s_setprio(2);
    STAMP(1);
    lds_wait(flags + F_Q);
    tag = flags[F_TAG];
    const v4u qv = *(LAS v4u*)(L + L_Q + (lane & 15) * 16);
    STAMP(2);
    float d[16];
#pragma unroll
    for (int i = 0; i < 16; ++i) {
      float a = dot2(K[i].x, qv.x, 0.f); a = dot2(K[i].y, qv.y, a); a = dot2(K[i].z, qv.z, a); a = dot2(K[i].w, qv.w, a);
      d[i] = a;
    }
    const int c = lane & 15;
#define TR_STEP(HALF, CTRL, BIT) _Pragma("unroll") for (int hh = 0; hh < (HALF); ++hh) { \
      const float lo_ = d[hh], hi_ = d[hh + (HALF)]; const float t1_ = lo_ + dppf<CTRL>(lo_), t2_ = hi_ + dppf<CTRL>(hi_); d[hh] = (c & (BIT)) ? t2_ : t1_; }
    TR_STEP(8, ROR8, 8) TR_STEP(4, HMIRROR, 4) TR_STEP(2, XOR2, 2) TR_STEP(1, XOR1, 1)
#undef TR_STEP
    const float s = rbf(rbf(d[0]) * 0.08838834764831845f);
    const float mw = wave_max(s);
    if (lane == 0) flags[F_MAX + wv] = __float_as_uint(mw);
    float M;
    {
      unsigned mm;
      for (unsigned spin = 0;; ++spin) {
        mm = flags[F_MAX + (lane & 15)];
        if (all_ok(mm != SENT) || spin > (SPIN_CAP << 4)) break;
        __builtin_amdgcn_s_sleep(1);
      }
      M = __uint_as_float(mm);
      M = fmaxf(M, dppf<ROR8>(M)); M = fmaxf(M, dppf<ROR4>(M)); M = fmaxf(M, dppf<ROR2>(M)); M = fmaxf(M, dppf<ROR1>(M));
    }
    const float p = __builtin_amdgcn_exp2f((s - M) * LOG2E);
    const float l = wave_sum(p);
    if (lane == 0) *(LAS float*)(L + L_ML + wv * 4) = l;
    const unsigned pp = pack2(p, p);
    unsigned mask[4];
#pragma unroll
    for (int t = 0; t < 4; ++t) mask[t] = (c < 8 && (c >> 1) == t) ? ((c & 1) ? 0xffff0000u : 0x0000ffffu) : 0u;
    STAMP(3);
    f32x4 acc = {0.f, 0.f, 0.f, 0.f};
#define PV_STEP(i) { const unsigned pb = __builtin_amdgcn_update_dpp(0u, pp, NEWBCAST + (i), 0xf, 0xf, true); \
      v4u a = {pb & mask[0], pb & mask[1], pb & mask[2], pb & mask[3]}; acc = mfma16(a, V[i], acc); }
    PV_STEP(0) PV_STEP(1) PV_STEP(2) PV_STEP(3) PV_STEP(4) PV_STEP(5) PV_STEP(6) PV_STEP(7)
    PV_STEP(8) PV_STEP(9) PV_STEP(10) PV_STEP(11) PV_STEP(12) PV_STEP(13) PV_STEP(14) PV_STEP(15)
#undef PV_STEP
    if (lane < 32) *(LAS f32x4*)(L + L_O + wv * 512 + (8 * c + 4 * (lane >> 4)) * 4) = acc;
    const int g4 = lane >> 4;
    if (lds_inc(L, F_CNT1) == (unsigned)(nu - 1)) {
      const int dp = 16 * (lane & 3) + 4 * ((lane >> 2) & 3) + (lane >> 4);
      float o0 = 0.f, o1 = 0.f;
#pragma unroll
      for (int u = 0; u < 9; ++u) { const f32x2 ov = *(LAS f32x2*)(L + L_O + (u < nu ? u : 0) * 512 + dp * 8); o0 += u < nu ? ov.x : 0.f; o1 += u < nu ? ov.y : 0.f; }
      float Ls = *(LAS float*)(L + L_ML + (lane & 15) * 4);
      Ls += dppf<ROR8>(Ls); Ls += dppf<ROR4>(Ls); Ls += dppf<ROR2>(Ls); Ls += dppf<ROR1>(Ls);
      v4u go = {__float_as_uint(o0), tag, __float_as_uint(o1), tag};
      __builtin_amdgcn_raw_buffer_store_b128(go, rs, WS_XO + pid * 1024 + lane * 16, 0, AUX_SC1);
      v4u gm = {__float_as_uint(M), tag, __float_as_uint(Ls), tag};
      __builtin_amdgcn_raw_buffer_store_b128(gm, rs, lane == 0 ? WS_XM + pid * 16 : OOB, 0, AUX_SC1);
      CFENCE();
      if (lane == 0) flags[F_PUB] = 1u;
    }
    STAMP(4);
    }
    const int g4 = lane >> 4;
    const f32x4 z4 = {0.f, 0.f, 0.f, 0.f};
    if (isT1 && wv < 8) {
      const int hh = wv >> 2, sh = (wv >> 1) & 1, ch = wv & 1, q = (lane >> 2) & 3, e = lane & 3;
      const int hd = 4 * g + 2 * (j & 1) + hh;
      const int xo = WS_XO + (hd * 8 + 4 * sh + q) * 1024 + (g4 * 4 + e) * 16;
      v2u go[5];
      int off[5];
#pragma unroll
      for (int k = 0; k < 4; ++k) { go[k] = v2u{0u, 0u}; off[k] = xo + k * 256; }
      go[4] = v2u{0u, 0u}; off[4] = WS_XM + (hd * 8 + q + 4 * (g4 & 1)) * 16;
      v4u t[5];
      gather_pass<5>(rs, t, off);
      __builtin_amdgcn_sched_barrier(0);
      for (unsigned spin = 0; flags[F_W] < 1u && spin < (SPIN_CAP << 4); ++spin) __builtin_amdgcn_s_sleep(1);
      CFENCE();
      v4u Bf[4][2];
#pragma unroll
      for (int ee = 0; ee < 4; ++ee)
#pragma unroll
        for (int cb = 0; cb < 2; ++cb) {
          LAS unsigned char* a0 = L + L_W + (16 * hh + 4 * ee + g4) * 1024 + ch * 512 + ((lane & 15) >> 2) * 64 + (16 * cb + 4 * (lane & 3)) * 2;
          const v2u lo = tr_read(a0), hi = tr_read(a0 + 4 * 64);
          Bf[ee][cb] = v4u{lo.x, lo.y, hi.x, hi.y};
        }
      __builtin_amdgcn_sched_barrier(0);
      const bool ok0 = gather_check<5>(t, go, off, tag);
      STAMP(9);
      if (!ok0) gather<5, T1_SLEEP>(rs, go, off, tag);
      STAMP(5);
      const float mm = __uint_as_float(go[4].x), ll = __uint_as_float(go[4].y);
      float Mx = fmaxf(mm, dppf<ROR4>(mm)); Mx = fmaxf(Mx, dppf<ROR8>(Mx)); Mx = x16_max(Mx);
      const float f = __builtin_amdgcn_exp2f((mm - Mx) * LOG2E);
      float Ls = f * ll; Ls += dppf<ROR4>(Ls); Ls += dppf<ROR8>(Ls); Ls = x16_sum(Ls);
      const float fn = f * rcp(Ls);
      const auto sw = __builtin_amdgcn_permlane16_swap(__float_as_uint(fn), __float_as_uint(fn), false, false);
      const float fo = __uint_as_float(sh ? sw[1] : sw[0]);
      v4u A;
#pragma unroll
      for (int k = 0; k < 4; ++k) A[k] = pack2(fo * __uint_as_float(go[k].x), fo * __uint_as_float(go[k].y));
      float y[2];
#pragma unroll
      for (int cb = 0; cb < 2; ++cb) {
        const f32x4 D0 = mfma16(A, Bf[0][cb], z4), D1 = mfma16(A, Bf[1][cb], z4), D2 = mfma16(A, Bf[2][cb], z4), D3 = mfma16(A, Bf[3][cb], z4);
        y[cb] = (D0[0] + D1[1]) + (D2[2] + D3[3]);
      }
      const float y0 = x32_sum(x16_sum(y[0])), y1 = x32_sum(x16_sum(y[1]));
      if (lane < 32) *(LAS float*)(L + L_RED + wv * 128 + lane * 4) = (lane < 16) ? y0 : y1;
      BAR();
      if (wv == 0) {
        const int cc = lane & 31;
        float z0 = 0.f, z1 = 0.f;
#pragma unroll
        for (int u = 0; u < 4; ++u) { const f32x2 r = *(LAS f32x2*)(L + L_RED + (2 * u + (cc >> 4)) * 128 + (cc & 15) * 8); z0 += r.x; z1 += r.y; }
        v4u gy = {__float_as_uint(z0), tag, __float_as_uint(z1), tag};
        __builtin_amdgcn_raw_buffer_store_b128(gy, rs, lane < 32 ? WS_GG + (2 * g + (j & 1)) * 4096 + (32 * (j >> 1) + lane) * 16 : lane == 32 ? WS_FL + (g * 16 + j) * 16 : OOB, 0, AUX_SC1);
      }
      STAMP(6);
    } else if (isM1 && wv < 8) {
      const int half = (lane >> 3) & 1, tt = (lane >> 2) & 1, e = lane & 3;
      const int pj = 32 * wv + 16 * tt + 4 * g4 + e;
      const unsigned xw = ((const GAS unsigned*)x)[pj];
      const unsigned bias = b1[32 * m1 + 8 * g4 + (lane & 7)];
      for (unsigned spin = 0; flags[F_W] < 2u && spin < (SPIN_CAP << 4); ++spin) __builtin_amdgcn_s_sleep(1);
      CFENCE();
      v4u B1[2][2], B2[4];
#pragma unroll
      for (int t2 = 0; t2 < 2; ++t2)
#pragma unroll
        for (int cb = 0; cb < 2; ++cb) {
          LAS unsigned char* a0 = L + L_W + (64 * wv + 32 * t2 + 8 * g4 + ((lane & 15) >> 2)) * 64 + (16 * cb + 4 * (lane & 3)) * 2;
          const v2u lo = tr_read(a0), hi = tr_read(a0 + 4 * 64);
          B1[t2][cb] = v4u{lo.x, lo.y, hi.x, hi.y};
        }
#pragma unroll
      for (int cb = 0; cb < 4; ++cb) {
        LAS unsigned char* a0 = L + L_W + 32768 + wv * 4096 + g4 * 1024 + (cb >> 1) * 512 + ((lane & 15) >> 2) * 64 + (16 * (cb & 1) + 4 * (lane & 3)) * 2;
        const v2u lo = tr_read(a0), hi = tr_read(a0 + 4 * 64);
        B2[cb] = v4u{lo.x, lo.y, hi.x, hi.y};
      }
      STAMP(8);
      v2u gy[8];
      int off[8];
#pragma unroll
      for (int k = 0; k < 8; ++k) { gy[k] = v2u{0u, 0u}; off[k] = WS_GG + (8 * half + k) * 4096 + pj * 16; }
      gather<8, 2>(rs, gy, off, tag);
      STAMP(5);
      float y0 = ((__uint_as_float(gy[0].x) + __uint_as_float(gy[1].x)) + (__uint_as_float(gy[2].x) + __uint_as_float(gy[3].x))) + ((__uint_as_float(gy[4].x) + __uint_as_float(gy[5].x)) + (__uint_as_float(gy[6].x) + __uint_as_float(gy[7].x)));
      float y1 = ((__uint_as_float(gy[0].y) + __uint_as_float(gy[1].y)) + (__uint_as_float(gy[2].y) + __uint_as_float(gy[3].y))) + ((__uint_as_float(gy[4].y) + __uint_as_float(gy[5].y)) + (__uint_as_float(gy[6].y) + __uint_as_float(gy[7].y)));
      y0 += dppf<ROR8>(y0); y1 += dppf<ROR8>(y1);
      const unsigned hp = pack2(bf2f_lo(xw) + rbf(y0), bf2f_hi(xw) + rbf(y1));
      const float h0 = bf2f_lo(hp), h1 = bf2f_hi(hp);
      if (m1 == 0) {
        v4u gh = {__float_as_uint(h0), tag, __float_as_uint(h1), tag};
        __builtin_amdgcn_raw_buffer_store_b128(gh, rs, half == 0 ? WS_P2 + 43 * 4096 + pj * 16 : OOB, 0, AUX_SC1);
      }
      {
        const float cp = __builtin_bit_cast(float, __builtin_amdgcn_readfirstlane(__builtin_bit_cast(int, h0)));
        const float d0 = h0 - cp, d1 = h1 - cp;
        float s1 = d0 + d1, s2 = fmaf(d0, d0, d1 * d1);
        s1 += dppf<XOR1>(s1); s2 += dppf<XOR1>(s2); s1 += dppf<XOR2>(s1); s2 += dppf<XOR2>(s2);
        s1 += dppf<HMIRROR>(s1); s2 += dppf<HMIRROR>(s2);
        s1 = x16_sum(s1); s2 = x16_sum(s2); s1 = x32_sum(s1); s2 = x32_sum(s2);
        const float mw = fmaf(s1, 1.f / 64.f, cp), m2w = fmaf(-s1 * (1.f / 64.f), s1, s2);
        if (lane == 0) *(LAS f32x2*)(L + L_ST + wv * 8) = f32x2{mw, m2w};
      }
      BAR();
      STAMP(9);
      float mb, inv;
      {
        const f32x2 st = *(LAS f32x2*)(L + L_ST + (lane & 7) * 8);
        const float mean = sum8(st.x) * 0.125f;
        const float dm = st.x - mean;
        const float var = rbf(fmaxf(sum8(fmaf(64.f * dm, dm, st.y)) * (1.f / 512.f), 0.f));
        mb = rbf(mean);
        inv = rcp(rbf(__builtin_amdgcn_sqrtf(rbf(var + 1e-5f))));
      }
      const unsigned hn = ln_apply(hp, mb, inv);
      const v4u A = {dppu<0x00>(hn), dppu<0x55>(hn), dppu<0xAA>(hn), dppu<0xFF>(hn)};
      const f32x4 D00 = mfma16(A, B1[0][0], z4), D10 = mfma16(A, B1[1][0], z4), D01 = mfma16(A, B1[0][1], z4), D11 = mfma16(A, B1[1][1], z4);
      const float r0 = x16_sum(g4 == 0 ? D00[0] : D10[0]), r1 = x16_sum(g4 == 0 ? D01[0] : D11[0]);
      if (lane < 16) { *(LAS float*)(L + L_RED + wv * 128 + lane * 4) = r0; *(LAS float*)(L + L_RED + wv * 128 + 64 + lane * 4) = r1; }
      BAR();
      float a = 0.f;
#pragma unroll
      for (int u = 0; u < 8; ++u) a += *(LAS float*)(L + L_RED + u * 128 + (8 * g4 + (lane & 7)) * 4);
      a = rbf(rbf(a) + bf2f_lo(bias));
      const float tq = 0.7978845608028654f * (a + 0.044715f * a * a * a);
      const float ex = __builtin_amdgcn_exp2f(tq * (2.f * LOG2E));
      const float th = 1.f - 2.f * rcp(ex + 1.f);
      const float ge = 0.5f * a * (1.f + th);
      const unsigned pr = pack2(ge, dppf<XOR1>(ge));
      const v4u A2 = {(unsigned)__builtin_amdgcn_update_dpp(0u, pr, NEWBCAST + 0, 0xf, 0xf, true), (unsigned)__builtin_amdgcn_update_dpp(0u, pr, NEWBCAST + 2, 0xf, 0xf, true),
                      (unsigned)__builtin_amdgcn_update_dpp(0u, pr, NEWBCAST + 4, 0xf, 0xf, true), (unsigned)__builtin_amdgcn_update_dpp(0u, pr, NEWBCAST + 6, 0xf, 0xf, true)};
      const f32x4 E0 = mfma16(A2, B2[0], z4), E1 = mfma16(A2, B2[1], z4), E2 = mfma16(A2, B2[2], z4), E3 = mfma16(A2, B2[3], z4);
      const float v0 = g4 == 0 ? E0[0] : g4 == 1 ? E1[0] : g4 == 2 ? E2[0] : E3[0];
      const float v1 = dppf<XOR1>(v0);
      v4u gp = {__float_as_uint(v0), tag, __float_as_uint(v1), tag};
      __builtin_amdgcn_raw_buffer_store_b128(gp, rs, (lane & 1) == 0 ? WS_P2 + m1 * 4096 + (32 * wv + 8 * g4 + ((lane & 15) >> 1)) * 16 : OOB, 0, AUX_SC1);
      STAMP(6);
    }
  }
  STAMP_FLUSH();
}
}

extern "C" void kernel_launch(void* const* d_in, const int* in_sizes, int n_in, void* d_out, int out_size, void* d_ws, size_t ws_size, hipStream_t stream) {
  (void)in_sizes; (void)n_in; (void)out_size; (void)ws_size;
  (void)hipFuncSetAttribute((const void*)fused_decode, hipFuncAttributeMaxDynamicSharedMemorySize, LDS_BYTES);
  hipLaunchKernelGGL(fused_decode, dim3(NWG), dim3(NTHR), LDS_BYTES, stream,
                     (const u16*)d_in[0], (const u16*)d_in[1], (const u16*)d_in[2], (const u16*)d_in[3], (const u16*)d_in[4],
                     (const u16*)d_in[6], (const u16*)d_in[7], (const u16*)d_in[8], (const u16*)d_in[9], (u16*)d_out, (unsigned char*)d_ws);
}
```

```cpp
#include <hip/hip_runtime.h>
#include <stdint.h>

typedef unsigned v4u __attribute__((ext_vector_type(4)));
typedef unsigned v2u __attribute__((ext_vector_type(2)));
typedef float f32x4 __attribute__((ext_vector_type(4)));
typedef float f32x2 __attribute__((ext_vector_type(2)));
typedef short v4s __attribute__((ext_vector_type(4)));
typedef __bf16 bf16x2 __attribute__((ext_vector_type(2)));
typedef __bf16 bf16x8 __attribute__((ext_vector_type(8)));
typedef unsigned short u16;
#define LAS __attribute__((address_space(3)))
#define GAS __attribute__((address_space(1)))

namespace {
constexpr int WINW = 4;
constexpr int THRN = 12;
constexpr int T1_SLEEP = 0;
constexpr int F_DELAY = 4;
constexpr int DPRIO0 = 2;
constexpr int DPRIO1 = 0;
constexpr int MPRIO0 = 0;
constexpr int MPRIO1 = 0;
constexpr int FLJ = 4;
constexpr int M1_SLEEP = 2;
constexpr int F_SLEEP = 2;
constexpr int KAUX = 2;
constexpr int VAUX = 2;
constexpr int DWIN = 4;
constexpr int DTHRN = 12;
constexpr int SK = 16384, DH = 128, WIN = 4096, FF = 1376;
#define NWAVES 10
constexpr int NWG = 256, NTHR = 64 * NWAVES;
constexpr int WS_EPOCH = 0;
constexpr int XMS = 128;
constexpr int WS_XM = 4096;
constexpr int WS_FL = WS_XM + 32768;
constexpr int WS_QG = WS_FL + 8192;
constexpr int WS_XO = WS_QG + 131072;
constexpr int WS_GG = WS_XO + 262144;
constexpr int WS_P2 = WS_GG + 65536;
constexpr int WS_END = WS_P2 + 44 * 4096;
constexpr int L_W = 0;
constexpr int L_Q = 65536;
constexpr int L_FLAG = L_Q + 256;
constexpr int L_ML = L_FLAG + 128;
constexpr int L_O = L_ML + 64;
constexpr int L_RED = L_O + 4608;
constexpr int L_ST = L_RED + 1024;
constexpr int L_END = L_ST + 128;
constexpr int LDS_BYTES = 84 * 1024;
static_assert(L_END <= LDS_BYTES, "lds");
constexpr int F_Q = 0, F_TAG = 1, F_CNT1 = 2, F_W = 4, F_PUB = 6, F_MAX = 8;
constexpr unsigned SENT = 0x7fffffffu;
constexpr int OOB = 0x7ffffff0;
constexpr int AUX_NT = 2, AUX_SC1 = 16;
constexpr unsigned SPIN_CAP = 1u << 16;
constexpr float LOG2E = 1.4426950408889634f;

__device__ __forceinline__ float bf2f_lo(unsigned u) { return __builtin_bit_cast(float, u << 16); }
__device__ __forceinline__ float bf2f_hi(unsigned u) { return __builtin_bit_cast(float, u & 0xffff0000u); }
__device__ __forceinline__ unsigned pack2(float a, float b) { bf16x2 r = {(__bf16)a, (__bf16)b}; return __builtin_bit_cast(unsigned, r); }
__device__ __forceinline__ float rbf(float f) { return bf2f_lo(pack2(f, f)); }
__device__ __forceinline__ float dot2(unsigned a, unsigned b, float acc) {
  return __builtin_amdgcn_fdot2_f32_bf16(__builtin_bit_cast(bf16x2, a), __builtin_bit_cast(bf16x2, b), acc, false);
}
template <int CTRL> __device__ __forceinline__ unsigned dppu(unsigned x) { return __builtin_amdgcn_mov_dpp(x, CTRL, 0xf, 0xf, true); }
template <int CTRL> __device__ __forceinline__ float dppf(float x) { return __builtin_bit_cast(float, dppu<CTRL>(__builtin_bit_cast(unsigned, x))); }
constexpr int XOR1 = 0xB1, XOR2 = 0x4E, HMIRROR = 0x141, ROR1 = 0x121, ROR2 = 0x122, ROR4 = 0x124, ROR8 = 0x128, NEWBCAST = 0x150;
__device__ __forceinline__ float x16_sum(float x) { auto s = __builtin_amdgcn_permlane16_swap(__float_as_uint(x), __float_as_uint(x), false, false); return __uint_as_float(s[0]) + __uint_as_float(s[1]); }
__device__ __forceinline__ float x32_sum(float x) { auto s = __builtin_amdgcn_permlane32_swap(__float_as_uint(x), __float_as_uint(x), false, false); return __uint_as_float(s[0]) + __uint_as_float(s[1]); }
__device__ __forceinline__ float x16_max(float x) { auto s = __builtin_amdgcn_permlane16_swap(__float_as_uint(x), __float_as_uint(x), false, false); return fmaxf(__uint_as_float(s[0]), __uint_as_float(s[1])); }
__device__ __forceinline__ float x32_max(float x) { auto s = __builtin_amdgcn_permlane32_swap(__float_as_uint(x), __float_as_uint(x), false, false); return fmaxf(__uint_as_float(s[0]), __uint_as_float(s[1])); }
__device__ __forceinline__ float wave_sum(float x) {
  x += dppf<ROR8>(x); x += dppf<ROR4>(x); x += dppf<ROR2>(x); x += dppf<ROR1>(x);
  return x32_sum(x16_sum(x));
}
__device__ __forceinline__ float wave_max(float x) {
  x = fmaxf(x, dppf<ROR8>(x)); x = fmaxf(x, dppf<ROR4>(x)); x = fmaxf(x, dppf<ROR2>(x)); x = fmaxf(x, dppf<ROR1>(x));
  return x32_max(x16_max(x));
}
__device__ __forceinline__ f32x4 mfma16(v4u a, v4u b, f32x4 c) {
  return __builtin_amdgcn_mfma_f32_16x16x32_bf16(__builtin_bit_cast(bf16x8, a), __builtin_bit_cast(bf16x8, b), c, 0, 0, 0);
}
__device__ __forceinline__ v2u tr_read(LAS unsigned char* p) { return __builtin_bit_cast(v2u, __builtin_amdgcn_ds_read_tr16_b64_v4i16((LAS v4s*)p)); }
#define LGKM0() asm volatile("s_waitcnt lgkmcnt(0)" ::: "memory")
#define VM0() asm volatile("s_waitcnt vmcnt(0)" ::: "memory")
#define BAR() do { asm volatile("s_waitcnt lgkmcnt(0)" ::: "memory"); __builtin_amdgcn_s_barrier(); asm volatile("" ::: "memory"); } while (0)
#define CFENCE() asm volatile("" ::: "memory")
__device__ __forceinline__ bool all_ok(bool ok) { return __builtin_amdgcn_ballot_w64(!ok) == 0ull; }

__device__ __forceinline__ void ln_stats(const float (&v)[8], float& mb, float& inv) {
  const float c = __builtin_bit_cast(float, __builtin_amdgcn_readfirstlane(__builtin_bit_cast(int, v[0])));
  float s = 0.f, q = 0.f;
#pragma unroll
  for (int j = 0; j < 8; ++j) { const float d = v[j] - c; s += d; q = fmaf(d, d, q); }
  s += dppf<ROR8>(s); q += dppf<ROR8>(q); s += dppf<ROR4>(s); q += dppf<ROR4>(q);
  s += dppf<ROR2>(s); q += dppf<ROR2>(q); s += dppf<ROR1>(s); q += dppf<ROR1>(q);
  s = x16_sum(s); q = x16_sum(q); s = x32_sum(s); q = x32_sum(q);
  const float ms = s * (1.f / 512.f);
  const float var = rbf(fmaxf(fmaf(-ms, ms, q * (1.f / 512.f)), 0.f));
  mb = rbf(c + ms);
  const float den = rbf(__builtin_amdgcn_sqrtf(rbf(var + 1e-5f)));
  inv = __builtin_amdgcn_rcpf(den);
}
__device__ __forceinline__ float rcp(float x) { return __builtin_amdgcn_rcpf(x); }
__device__ __forceinline__ unsigned ln_apply(unsigned pr, float mb, float inv) { return pack2(rbf(bf2f_lo(pr) - mb) * inv, rbf(bf2f_hi(pr) - mb) * inv); }
__device__ __forceinline__ float sum8(float x) { x += dppf<XOR1>(x); x += dppf<XOR2>(x); x += dppf<HMIRROR>(x); return x; }
__device__ __forceinline__ float max8(float x) { x = fmaxf(x, dppf<XOR1>(x)); x = fmaxf(x, dppf<XOR2>(x)); x = fmaxf(x, dppf<HMIRROR>(x)); return x; }
template <int N> __device__ __forceinline__ void gather_pass(__amdgpu_buffer_rsrc_t rs, v4u (&t)[N], const int (&off)[N]) {
  CFENCE();
#pragma unroll
  for (int k = 0; k < N; ++k) t[k] = __builtin_amdgcn_raw_buffer_load_b128(rs, off[k], 0, AUX_SC1);
}
template <int N> __device__ __forceinline__ bool gather_check(const v4u (&t)[N], v2u (&g)[N], int (&off)[N], unsigned tag) {
  bool all = true;
#pragma unroll
  for (int k = 0; k < N; ++k) {
    const bool live = off[k] != OOB;
    const bool ok = live && t[k].y == tag && t[k].w == tag;
    g[k].x = ok ? t[k].x : g[k].x; g[k].y = ok ? t[k].z : g[k].y;
    off[k] = ok ? OOB : off[k];
    all = all && (ok || !live);
  }
  return all_ok(all);
}
template <int N, int SLEEP = 0> __device__ __forceinline__ void gather(__amdgpu_buffer_rsrc_t rs, v2u (&g)[N], int (&off)[N], unsigned tag) {
  for (unsigned spin = 0;; ++spin) {
    v4u t[N];
    gather_pass<N>(rs, t, off);
    if (gather_check<N>(t, g, off, tag) || spin > SPIN_CAP) break;
    if (SLEEP) __builtin_amdgcn_s_sleep(SLEEP);
  }
}
__device__ __forceinline__ void lds_wait(volatile LAS unsigned* f) {
  for (unsigned spin = 0; *f == 0u && spin < (SPIN_CAP << 4); ++spin) __builtin_amdgcn_s_sleep(1);
  CFENCE();
}
__device__ __forceinline__ unsigned lds_inc(LAS unsigned char* L, int w) {
  unsigned r = 0;
  asm volatile("" ::: "memory");
  if ((threadIdx.x & 63) == 0) r = __hip_atomic_fetch_add((LAS unsigned*)(L + L_FLAG + w * 4), 1u, __ATOMIC_RELAXED, __HIP_MEMORY_SCOPE_WORKGROUP);
  r = __builtin_amdgcn_readfirstlane(r);
  asm volatile("" ::: "memory");
  return r;
}

template <bool NT> __device__ __forceinline__ void dma16(__amdgpu_buffer_rsrc_t rs, int voff, int soff, unsigned lds) {
  if (NT) asm volatile("s_mov_b32 m0, %0\n\ts_nop 0\n\tbuffer_load_dwordx4 %1, %2, %3 offen nt lds" :: "s"(lds), "v"(voff), "s"(rs), "s"(soff) : "m0", "memory");
  else asm volatile("s_mov_b32 m0, %0\n\ts_nop 0\n\tbuffer_load_dwordx4 %1, %2, %3 offen lds" :: "s"(lds), "v"(voff), "s"(rs), "s"(soff) : "m0", "memory");
}

__device__ __forceinline__ void weight_job(int job, LAS unsigned char* L, const u16* wo, const u16* w1, const u16* w2, int g, int j, int m1, int lane) {
  const bool isT1 = job == 0, isw1 = job == 1;
  const int tl = ((lane >> 2) & 7), tb = (lane >> 5) * 64 + (lane & 3) * 16;
  const void* wp = isT1 ? (const void*)wo : isw1 ? (const void*)w1 : (const void*)w2;
  const __amdgpu_buffer_rsrc_t rw = __builtin_amdgcn_make_buffer_rsrc((void*)wp, 0, isT1 ? 4096 * 1024 : FF * 1024, 0x00020000);
  const int vo = isT1 ? ((4 * g + 2 * (j & 1)) * 128 + tl) * 1024 + 128 * (j >> 1) + tb
               : isw1 ? (lane >> 2) * (FF * 2) + 64 * m1 + (lane & 3) * 16 : (32 * m1 + tl) * 1024 + tb;
  const int s1 = isT1 ? 8192 : isw1 ? 16 * FF * 2 : 8192, s4 = isT1 ? 4 * 8192 : isw1 ? 4 * 16 * FF * 2 : 128;
  const unsigned lb = (unsigned)(uintptr_t)(L + L_W) + (job == 2 ? 32768 : 0);
#pragma unroll
  for (int i = 0; i < 32; ++i) {
    if (i == 0) { if (isT1) __builtin_amdgcn_s_setprio(DPRIO0); else __builtin_amdgcn_s_setprio(MPRIO0); }
    if (i >= DWIN && i < DTHRN && isT1) { __builtin_amdgcn_sched_barrier(0); asm volatile("s_waitcnt vmcnt(%0)" :: "n"(DWIN - 1)); __builtin_amdgcn_sched_barrier(0); }
    if (i == 16) { if (isT1) __builtin_amdgcn_s_setprio(DPRIO1); else __builtin_amdgcn_s_setprio(MPRIO1); }
    dma16<true>(rw, vo, (i & 3) * s1 + (i >> 2) * s4, lb + i * 1024);
  }
}


__global__ void __launch_bounds__(NTHR, 3)
fused_decode(const u16* __restrict__ x, const u16* __restrict__ kc, const u16* __restrict__ vc, const u16* __restrict__ wq, const u16* __restrict__ wo,
             const u16* __restrict__ w1, const u16* __restrict__ b1, const u16* __restrict__ w2, const u16* __restrict__ b2, u16* __restrict__ out, unsigned char* ws) {
  extern __shared__ __attribute__((aligned(16))) unsigned char smem[];
  LAS unsigned char* L = (LAS unsigned char*)smem;
  const int tid = threadIdx.x, lane = tid & 63, wave = __builtin_amdgcn_readfirstlane(tid >> 6);
  const int b = blockIdx.x, g = b & 7, j = b >> 3;
  const int head = 4 * g + (j & 3), sp = j >> 2;
  const int pid = head * 8 + sp;
  constexpr int nu = 8;
  const int jr = j - 16;
  const bool isT1 = j < 16;
  const int m1 = g * 6 + jr;
  const bool isM1 = (jr >= 0) && (jr < 6) && (m1 < 43);
  const int m2 = g * 8 + (jr - 6);
  const bool isF = (jr >= 6) && (jr < 14);
  const __amdgpu_buffer_rsrc_t rs = __builtin_amdgcn_make_buffer_rsrc((void*)ws, 0, WS_END, 0x00020000);
  volatile LAS unsigned* flags = (volatile LAS unsigned*)(L + L_FLAG);

  if (wave == 0) {
    __builtin_amdgcn_s_setprio(3);
    const __amdgpu_buffer_rsrc_t rx = __builtin_amdgcn_make_buffer_rsrc((void*)x, 0, 1024, 0x00020000);
    const v4u xv = __builtin_amdgcn_raw_buffer_load_b128(rx, lane * 16, 0, 0);
    __builtin_amdgcn_sched_barrier(0);
    v4u wv[16];
    const int rq = sp >> 1, ch = sp & 1;
    {
      const __amdgpu_buffer_rsrc_t rw = __builtin_amdgcn_make_buffer_rsrc((void*)wq, 0, 512 * 8192, 0x00020000);
      const int vo = (rq * 128 + (lane >> 3)) * 8192 + (head * 128 + 64 * ch + 8 * (lane & 7)) * 2;
#pragma unroll
      for (int i = 0; i < 16; ++i) wv[i] = __builtin_amdgcn_raw_buffer_load_b128(rw, vo, i * 8 * 8192, AUX_NT);
    }
    __builtin_amdgcn_sched_barrier(0);
    const unsigned epv = __builtin_amdgcn_raw_buffer_load_b32(rs, WS_EPOCH, 0, AUX_SC1);
    if (lane < 48) flags[lane] = (lane >= F_MAX && lane < F_MAX + 16) ? (lane < F_MAX + nu ? SENT : 0xff800000u) : 0u;
    __builtin_amdgcn_sched_barrier(0);
    BAR();
    __builtin_amdgcn_sched_barrier(0);
    float xf[8];
#pragma unroll
    for (int k = 0; k < 4; ++k) { xf[2 * k] = bf2f_lo(xv[k]); xf[2 * k + 1] = bf2f_hi(xv[k]); }
    float mb, inv;
    ln_stats(xf, mb, inv);
    {
      LAS u16* T = (LAS u16*)(L + L_O + (lane >> 4) * 256);
#pragma unroll
      for (int k = 0; k < 4; ++k) {
        const unsigned xn = ln_apply(xv[k], mb, inv);
        T[(2 * k) * 16 + (lane & 15)] = (u16)xn;
        T[(2 * k + 1) * 16 + (lane & 15)] = (u16)(xn >> 16);
      }
    }
    LGKM0();
    const v4u xt0 = *(LAS v4u*)(L + L_O + rq * 256 + (lane >> 3) * 32), xt1 = *(LAS v4u*)(L + L_O + rq * 256 + (lane >> 3) * 32 + 16);
    float acc[8];
#pragma unroll
    for (int k = 0; k < 8; ++k) acc[k] = 0.f;
#pragma unroll
    for (int i = 0; i < 16; ++i) {
      const unsigned src = (i < 8) ? xt0[(i & 7) >> 1] : xt1[(i & 7) >> 1];
      const unsigned lo = (i & 1) ? (src >> 16) : (src & 0xffffu), hi = (i & 1) ? (src & 0xffff0000u) : (src << 16);
#pragma unroll
      for (int k = 0; k < 4; ++k) { acc[2 * k] = dot2(wv[i][k], lo, acc[2 * k]); acc[2 * k + 1] = dot2(wv[i][k], hi, acc[2 * k + 1]); }
    }
#pragma unroll
    for (int k = 0; k < 8; ++k) { float a = acc[k]; a += dppf<ROR8>(a); acc[k] = x32_sum(x16_sum(a)); }
    const unsigned tag = (unsigned)__builtin_amdgcn_readfirstlane((int)epv) + 1u;
    {
      const int k = (lane >> 3) & 3;
      const float q0 = k == 0 ? acc[0] : k == 1 ? acc[2] : k == 2 ? acc[4] : acc[6];
      const float q1 = k == 0 ? acc[1] : k == 1 ? acc[3] : k == 2 ? acc[5] : acc[7];
      v4u gq = {__float_as_uint(q0), tag, __float_as_uint(q1), tag};
      __builtin_amdgcn_raw_buffer_store_b128(gq, rs, lane < 32 ? WS_QG + head * 4096 + (ch * 4 + rq) * 512 + lane * 16 : OOB, 0, AUX_SC1);
    }
    if (lane == 0) flags[F_TAG] = tag;
    {
      v2u gq[4];
      int off[4];
#pragma unroll
      for (int k = 0; k < 4; ++k) { gq[k] = v2u{0u, 0u}; off[k] = WS_QG + head * 4096 + ((lane >> 5) * 4 + k) * 512 + (lane & 31) * 16; }
      gather<4>(rs, gq, off, tag);
      const float q0 = (__uint_as_float(gq[0].x) + __uint_as_float(gq[1].x)) + (__uint_as_float(gq[2].x) + __uint_as_float(gq[3].x));
      const float q1 = (__uint_as_float(gq[0].y) + __uint_as_float(gq[1].y)) + (__uint_as_float(gq[2].y) + __uint_as_float(gq[3].y));
      *(LAS unsigned*)(L + L_Q + ((lane >> 5) * 32 + 4 * (lane & 7) + ((lane >> 3) & 3)) * 4) = pack2(q0, q1);
      LGKM0();
      if (lane == 0) flags[F_Q] = 1u;
    }
    if (isM1) {
      lds_wait(flags + F_PUB);
      weight_job(2, L, wo, w1, w2, g, j, m1, lane);
      VM0();
      lds_inc(L, F_W);
    }
    if (isF) {
      const unsigned xb = ((const GAS unsigned*)b2)[4 * m2 + (lane & 3)];
      {
        for (unsigned spin = 0; flags[F_CNT1] < (unsigned)nu && spin < (SPIN_CAP << 4); ++spin) __builtin_amdgcn_s_sleep(4);
        v2u gm[1] = {v2u{0u, 0u}};
        int om[1] = {lane < 8 * FLJ ? WS_FL + lane * 128 : OOB};
        gather<1, 4>(rs, gm, om, tag);
        __builtin_amdgcn_s_sleep(F_DELAY);
      }
      v2u gy[3];
      int off[3];
#pragma unroll
      for (int k = 0; k < 3; ++k) { gy[k] = v2u{0u, 0u}; const int pr = 16 * k + (lane >> 2); off[k] = pr < 44 ? WS_P2 + pr * 4096 + (4 * m2 + (lane & 3)) * 16 : OOB; }
      gather<3, F_SLEEP>(rs, gy, off, tag);
      const bool ish = (lane >> 2) == 11;
      float y0 = __uint_as_float(gy[0].x) + __uint_as_float(gy[1].x) + (ish ? 0.f : __uint_as_float(gy[2].x));
      float y1 = __uint_as_float(gy[0].y) + __uint_as_float(gy[1].y) + (ish ? 0.f : __uint_as_float(gy[2].y));
      float h0 = ish ? __uint_as_float(gy[2].x) : 0.f, h1 = ish ? __uint_as_float(gy[2].y) : 0.f;
      y0 += dppf<ROR4>(y0); y1 += dppf<ROR4>(y1); h0 += dppf<ROR4>(h0); h1 += dppf<ROR4>(h1);
      y0 += dppf<ROR8>(y0); y1 += dppf<ROR8>(y1); h0 += dppf<ROR8>(h0); h1 += dppf<ROR8>(h1);
      y0 = x32_sum(x16_sum(y0)); y1 = x32_sum(x16_sum(y1)); h0 = x32_sum(x16_sum(h0)); h1 = x32_sum(x16_sum(h1));
      const unsigned pr = pack2(h0 + rbf(rbf(y0) + bf2f_lo(xb)), h1 + rbf(rbf(y1) + bf2f_hi(xb)));
      if (lane < 4) ((GAS unsigned*)out)[4 * m2 + lane] = pr;
      if (m2 == 0 && lane == 0) __builtin_amdgcn_raw_buffer_store_b32(tag, rs, WS_EPOCH, 0, AUX_SC1);
    }
  } else {
    const int wv = wave - 1;
    const int rot = b * 5 + (b >> 3) * 3;
    __builtin_amdgcn_s_setprio(2);
    BAR();
#define THROTTLE() do { __builtin_amdgcn_sched_barrier(0); asm volatile("s_waitcnt vmcnt(%0)" :: "n"(WINW - 1)); __builtin_amdgcn_sched_barrier(0); } while (0)
    unsigned tag = 0;
    if (wv >= nu) {
      if (isT1 || isM1) {
        const int job = isT1 ? 0 : 1;
        if (isM1) lds_wait(flags + F_PUB);
        weight_job(job, L, wo, w1, w2, g, j, m1, lane);
        VM0();
        lds_inc(L, F_W);
        __builtin_amdgcn_s_setprio(2);
        lds_wait(flags + F_Q);
        tag = flags[F_TAG];
      }
    } else {
    const size_t base = ((size_t)head * SK + (SK - WIN) + (wv * 8 + sp) * 64) * (DH * 2);
    const int span = 16384;
    constexpr int BLK = 1024;
    const __amdgpu_buffer_rsrc_t rk = __builtin_amdgcn_make_buffer_rsrc((void*)((const char*)kc + base), 0, span, 0x00020000);
    const __amdgpu_buffer_rsrc_t rv = __builtin_amdgcn_make_buffer_rsrc((void*)((const char*)vc + base), 0, span, 0x00020000);
    v4u K[16], V[16];
#pragma unroll
    for (int i = 0; i < 16; ++i) { if (i >= WINW && i < THRN) THROTTLE();
      K[i] = __builtin_amdgcn_raw_buffer_load_b128(rk, lane * 16, ((i + rot) & 15) * BLK, KAUX); }
    __builtin_amdgcn_sched_barrier(0);
    __builtin_amdgcn_s_setprio(0);
    __builtin_amdgcn_sched_barrier(0);
#pragma unroll
    for (int i = 0; i < 16; ++i) V[i] = __builtin_amdgcn_raw_buffer_load_b128(rv, lane * 16, ((i + rot) & 15) * BLK, VAUX);
    __builtin_amdgcn_sched_barrier(0);
    __builtin_amdgcn_s_setprio(2);
    lds_wait(flags + F_Q);
    tag = flags[F_TAG];
    const v4u qv = *(LAS v4u*)(L + L_Q + (lane & 15) * 16);
    float d[16];
#pragma unroll
    for (int i = 0; i < 16; ++i) {
      float a = dot2(K[i].x, qv.x, 0.f); a = dot2(K[i].y, qv.y, a); a = dot2(K[i].z, qv.z, a); a = dot2(K[i].w, qv.w, a);
      d[i] = a;
    }
    const int c = lane & 15;
#define TR_STEP(HALF, CTRL, BIT) _Pragma("unroll") for (int hh = 0; hh < (HALF); ++hh) { \
      const float lo_ = d[hh], hi_ = d[hh + (HALF)]; const float t1_ = lo_ + dppf<CTRL>(lo_), t2_ = hi_ + dppf<CTRL>(hi_); d[hh] = (c & (BIT)) ? t2_ : t1_; }
    TR_STEP(8, ROR8, 8) TR_STEP(4, HMIRROR, 4) TR_STEP(2, XOR2, 2) TR_STEP(1, XOR1, 1)
#undef TR_STEP
    const float s = rbf(rbf(d[0]) * 0.08838834764831845f);
    const float mw = wave_max(s);
    if (lane == 0) flags[F_MAX + wv] = __float_as_uint(mw);
    float M;
    {
      unsigned mm;
      for (unsigned spin = 0;; ++spin) {
        mm = flags[F_MAX + (lane & 15)];
        if (all_ok(mm != SENT) || spin > (SPIN_CAP << 4)) break;
        __builtin_amdgcn_s_sleep(1);
      }
      M = __uint_as_float(mm);
      M = fmaxf(M, dppf<ROR8>(M)); M = fmaxf(M, dppf<ROR4>(M)); M = fmaxf(M, dppf<ROR2>(M)); M = fmaxf(M, dppf<ROR1>(M));
    }
    const float p = __builtin_amdgcn_exp2f((s - M) * LOG2E);
    const float l = wave_sum(p);
    if (lane == 0) *(LAS float*)(L + L_ML + wv * 4) = l;
    const unsigned pp = pack2(p, p);
    unsigned mask[4];
#pragma unroll
    for (int t = 0; t < 4; ++t) mask[t] = (c < 8 && (c >> 1) == t) ? ((c & 1) ? 0xffff0000u : 0x0000ffffu) : 0u;
    f32x4 acc = {0.f, 0.f, 0.f, 0.f};
#define PV_STEP(i) { const unsigned pb = __builtin_amdgcn_update_dpp(0u, pp, NEWBCAST + (i), 0xf, 0xf, true); \
      v4u a = {pb & mask[0], pb & mask[1], pb & mask[2], pb & mask[3]}; acc = mfma16(a, V[i], acc); }
    PV_STEP(0) PV_STEP(1) PV_STEP(2) PV_STEP(3) PV_STEP(4) PV_STEP(5) PV_STEP(6) PV_STEP(7)
    PV_STEP(8) PV_STEP(9) PV_STEP(10) PV_STEP(11) PV_STEP(12) PV_STEP(13) PV_STEP(14) PV_STEP(15)
#undef PV_STEP
    if (lane < 32) *(LAS f32x4*)(L + L_O + wv * 512 + (8 * c + 4 * (lane >> 4)) * 4) = acc;
    const int g4 = lane >> 4;
    if (lds_inc(L, F_CNT1) == (unsigned)(nu - 1)) {
      const int dp = 16 * (lane & 3) + 4 * ((lane >> 2) & 3) + (lane >> 4);
      float o0 = 0.f, o1 = 0.f;
#pragma unroll
      for (int u = 0; u < 9; ++u) { const f32x2 ov = *(LAS f32x2*)(L + L_O + (u < nu ? u : 0) * 512 + dp * 8); o0 += u < nu ? ov.x : 0.f; o1 += u < nu ? ov.y : 0.f; }
      float Ls = *(LAS float*)(L + L_ML + (lane & 15) * 4);
      Ls += dppf<ROR8>(Ls); Ls += dppf<ROR4>(Ls); Ls += dppf<ROR2>(Ls); Ls += dppf<ROR1>(Ls);
      v4u go = {__float_as_uint(o0), tag, __float_as_uint(o1), tag};
      __builtin_amdgcn_raw_buffer_store_b128(go, rs, WS_XO + pid * 1024 + lane * 16, 0, AUX_SC1);
      v4u gm = {__float_as_uint(M), tag, __float_as_uint(Ls), tag};
      __builtin_amdgcn_raw_buffer_store_b128(gm, rs, lane < 8 ? WS_XM + pid * XMS + lane * 16 : OOB, 0, AUX_SC1);
      CFENCE();
      if (lane == 0) flags[F_PUB] = 1u;
    }
    }
    const int g4 = lane >> 4;
    const f32x4 z4 = {0.f, 0.f, 0.f, 0.f};
    if (isT1 && wv < 8) {
      const int hh = wv >> 2, sh = (wv >> 1) & 1, ch = wv & 1, q = (lane >> 2) & 3, e = lane & 3;
      const int hd = 4 * g + 2 * (j & 1) + hh;
      const int xo = WS_XO + (hd * 8 + 4 * sh + q) * 1024 + (g4 * 4 + e) * 16;
      v2u go[5];
      int off[5];
#pragma unroll
      for (int k = 0; k < 4; ++k) { go[k] = v2u{0u, 0u}; off[k] = xo + k * 256; }
      go[4] = v2u{0u, 0u}; off[4] = WS_XM + (hd * 8 + q + 4 * (g4 & 1)) * XMS + e * 16;
      v4u t[5];
      gather_pass<5>(rs, t, off);
      __builtin_amdgcn_sched_barrier(0);
      for (unsigned spin = 0; flags[F_W] < 1u && spin < (SPIN_CAP << 4); ++spin) __builtin_amdgcn_s_sleep(1);
      CFENCE();
      v4u Bf[4][2];
#pragma unroll
      for (int ee = 0; ee < 4; ++ee)
#pragma unroll
        for (int cb = 0; cb < 2; ++cb) {
          LAS unsigned char* a0 = L + L_W + (16 * hh + 4 * ee + g4) * 1024 + ch * 512 + ((lane & 15) >> 2) * 64 + (16 * cb + 4 * (lane & 3)) * 2;
          const v2u lo = tr_read(a0), hi = tr_read(a0 + 4 * 64);
          Bf[ee][cb] = v4u{lo.x, lo.y, hi.x, hi.y};
        }
      __builtin_amdgcn_sched_barrier(0);
      const bool ok0 = gather_check<5>(t, go, off, tag);
      if (!ok0) gather<5, T1_SLEEP>(rs, go, off, tag);
      const float mm = __uint_as_float(go[4].x), ll = __uint_as_float(go[4].y);
      float Mx = fmaxf(mm, dppf<ROR4>(mm)); Mx = fmaxf(Mx, dppf<ROR8>(Mx)); Mx = x16_max(Mx);
      const float f = __builtin_amdgcn_exp2f((mm - Mx) * LOG2E);
      float Ls = f * ll; Ls += dppf<ROR4>(Ls); Ls += dppf<ROR8>(Ls); Ls = x16_sum(Ls);
      const float fn = f * rcp(Ls);
      const auto sw = __builtin_amdgcn_permlane16_swap(__float_as_uint(fn), __float_as_uint(fn), false, false);
      const float fo = __uint_as_float(sh ? sw[1] : sw[0]);
      v4u A;
#pragma unroll
      for (int k = 0; k < 4; ++k) A[k] = pack2(fo * __uint_as_float(go[k].x), fo * __uint_as_float(go[k].y));
      float y[2];
#pragma unroll
      for (int cb = 0; cb < 2; ++cb) {
        const f32x4 D0 = mfma16(A, Bf[0][cb], z4), D1 = mfma16(A, Bf[1][cb], z4), D2 = mfma16(A, Bf[2][cb], z4), D3 = mfma16(A, Bf[3][cb], z4);
        y[cb] = (D0[0] + D1[1]) + (D2[2] + D3[3]);
      }
      const float y0 = x32_sum(x16_sum(y[0])), y1 = x32_sum(x16_sum(y[1]));
      if (lane < 32) *(LAS float*)(L + L_RED + wv * 128 + lane * 4) = (lane < 16) ? y0 : y1;
      BAR();
      if (wv == 0) {
        const int cc = lane & 31;
        float z0 = 0.f, z1 = 0.f;
#pragma unroll
        for (int u = 0; u < 4; ++u) { const f32x2 r = *(LAS f32x2*)(L + L_RED + (2 * u + (cc >> 4)) * 128 + (cc & 15) * 8); z0 += r.x; z1 += r.y; }
        v4u gy = {__float_as_uint(z0), tag, __float_as_uint(z1), tag};
        __builtin_amdgcn_raw_buffer_store_b128(gy, rs, lane < 32 ? WS_GG + (2 * g + (j & 1)) * 4096 + (32 * (j >> 1) + lane) * 16 : (lane < 40 && j < FLJ) ? WS_FL + (g * FLJ + j) * 128 + (lane - 32) * 16 : OOB, 0, AUX_SC1);
      }
    } else if (isM1 && wv < 8) {
      const int half = (lane >> 3) & 1, tt = (lane >> 2) & 1, e = lane & 3;
      const int pj = 32 * wv + 16 * tt + 4 * g4 + e;
      const unsigned xw = ((const GAS unsigned*)x)[pj];
      const unsigned bias = b1[32 * m1 + 8 * g4 + (lane & 7)];
      v2u gy[8];
      int off[8];
#pragma unroll
      for (int k = 0; k < 8; ++k) { gy[k] = v2u{0u, 0u}; off[k] = WS_GG + (8 * half + k) * 4096 + pj * 16; }
      for (unsigned spin = 0; flags[F_W] < 2u && spin < (SPIN_CAP << 4); ++spin) __builtin_amdgcn_s_sleep(1);
      CFENCE();
      v4u B1[2][2], B2[4];
#pragma unroll
      for (int t2 = 0; t2 < 2; ++t2)
#pragma unroll
        for (int cb = 0; cb < 2; ++cb) {
          LAS unsigned char* a0 = L + L_W + (64 * wv + 32 * t2 + 8 * g4 + ((lane & 15) >> 2)) * 64 + (16 * cb + 4 * (lane & 3)) * 2;
          const v2u lo = tr_read(a0), hi = tr_read(a0 + 4 * 64);
          B1[t2][cb] = v4u{lo.x, lo.y, hi.x, hi.y};
        }
#pragma unroll
      for (int cb = 0; cb < 4; ++cb) {
        LAS unsigned char* a0 = L + L_W + 32768 + wv * 4096 + g4 * 1024 + (cb >> 1) * 512 + ((lane & 15) >> 2) * 64 + (16 * (cb & 1) + 4 * (lane & 3)) * 2;
        const v2u lo = tr_read(a0), hi = tr_read(a0 + 4 * 64);
        B2[cb] = v4u{lo.x, lo.y, hi.x, hi.y};
      }
      __builtin_amdgcn_sched_barrier(0);
      gather<8, M1_SLEEP>(rs, gy, off, tag);
      float y0 = ((__uint_as_float(gy[0].x) + __uint_as_float(gy[1].x)) + (__uint_as_float(gy[2].x) + __uint_as_float(gy[3].x))) + ((__uint_as_float(gy[4].x) + __uint_as_float(gy[5].x)) + (__uint_as_float(gy[6].x) + __uint_as_float(gy[7].x)));
      float y1 = ((__uint_as_float(gy[0].y) + __uint_as_float(gy[1].y)) + (__uint_as_float(gy[2].y) + __uint_as_float(gy[3].y))) + ((__uint_as_float(gy[4].y) + __uint_as_float(gy[5].y)) + (__uint_as_float(gy[6].y) + __uint_as_float(gy[7].y)));
      y0 += dppf<ROR8>(y0); y1 += dppf<ROR8>(y1);
      const unsigned hp = pack2(bf2f_lo(xw) + rbf(y0), bf2f_hi(xw) + rbf(y1));
      const float h0 = bf2f_lo(hp), h1 = bf2f_hi(hp);
      if (m1 == 0) {
        v4u gh = {__float_as_uint(h0), tag, __float_as_uint(h1), tag};
        __builtin_amdgcn_raw_buffer_store_b128(gh, rs, half == 0 ? WS_P2 + 43 * 4096 + pj * 16 : OOB, 0, AUX_SC1);
      }
      {
        const float cp = __builtin_bit_cast(float, __builtin_amdgcn_readfirstlane(__builtin_bit_cast(int, h0)));
        const float d0 = h0 - cp, d1 = h1 - cp;
        float s1 = d0 + d1, s2 = fmaf(d0, d0, d1 * d1);
        s1 += dppf<XOR1>(s1); s2 += dppf<XOR1>(s2); s1 += dppf<XOR2>(s1); s2 += dppf<XOR2>(s2);
        s1 += dppf<HMIRROR>(s1); s2 += dppf<HMIRROR>(s2);
        s1 = x16_sum(s1); s2 = x16_sum(s2); s1 = x32_sum(s1); s2 = x32_sum(s2);
        const float mw = fmaf(s1, 1.f / 64.f, cp), m2w = fmaf(-s1 * (1.f / 64.f), s1, s2);
        if (lane == 0) *(LAS f32x2*)(L + L_ST + wv * 8) = f32x2{mw, m2w};
      }
      BAR();
      float mb, inv;
      {
        const f32x2 st = *(LAS f32x2*)(L + L_ST + (lane & 7) * 8);
        const float mean = sum8(st.x) * 0.125f;
        const float dm = st.x - mean;
        const float var = rbf(fmaxf(sum8(fmaf(64.f * dm, dm, st.y)) * (1.f / 512.f), 0.f));
        mb = rbf(mean);
        inv = rcp(rbf(__builtin_amdgcn_sqrtf(rbf(var + 1e-5f))));
      }
      const unsigned hn = ln_apply(hp, mb, inv);
      const v4u A = {dppu<0x00>(hn), dppu<0x55>(hn), dppu<0xAA>(hn), dppu<0xFF>(hn)};
      const f32x4 D00 = mfma16(A, B1[0][0], z4), D10 = mfma16(A, B1[1][0], z4), D01 = mfma16(A, B1[0][1], z4), D11 = mfma16(A, B1[1][1], z4);
      const float r0 = x16_sum(g4 == 0 ? D00[0] : D10[0]), r1 = x16_sum(g4 == 0 ? D01[0] : D11[0]);
      if (lane < 16) { *(LAS float*)(L + L_RED + wv * 128 + lane * 4) = r0; *(LAS float*)(L + L_RED + wv * 128 + 64 + lane * 4) = r1; }
      BAR();
      float a = 0.f;
#pragma unroll
      for (int u = 0; u < 8; ++u) a += *(LAS float*)(L + L_RED + u * 128 + (8 * g4 + (lane & 7)) * 4);
      a = rbf(rbf(a) + bf2f_lo(bias));
      const float tq = 0.7978845608028654f * (a + 0.044715f * a * a * a);
      const float ex = __builtin_amdgcn_exp2f(tq * (2.f * LOG2E));
      const float th = 1.f - 2.f * rcp(ex + 1.f);
      const float ge = 0.5f * a * (1.f + th);
      const unsigned pr = pack2(ge, dppf<XOR1>(ge));
      const v4u A2 = {(unsigned)__builtin_amdgcn_update_dpp(0u, pr, NEWBCAST + 0, 0xf, 0xf, true), (unsigned)__builtin_amdgcn_update_dpp(0u, pr, NEWBCAST + 2, 0xf, 0xf, true),
                      (unsigned)__builtin_amdgcn_update_dpp(0u, pr, NEWBCAST + 4, 0xf, 0xf, true), (unsigned)__builtin_amdgcn_update_dpp(0u, pr, NEWBCAST + 6, 0xf, 0xf, true)};
      const f32x4 E0 = mfma16(A2, B2[0], z4), E1 = mfma16(A2, B2[1], z4), E2 = mfma16(A2, B2[2], z4), E3 = mfma16(A2, B2[3], z4);
      const float v0 = g4 == 0 ? E0[0] : g4 == 1 ? E1[0] : g4 == 2 ? E2[0] : E3[0];
      const float v1 = dppf<XOR1>(v0);
      v4u gp = {__float_as_uint(v0), tag, __float_as_uint(v1), tag};
      __builtin_amdgcn_raw_buffer_store_b128(gp, rs, (lane & 1) == 0 ? WS_P2 + m1 * 4096 + (32 * wv + 8 * g4 + ((lane & 15) >> 1)) * 16 : OOB, 0, AUX_SC1);
    }
  }
}
}

extern "C" void kernel_launch(void* const* d_in, const int* in_sizes, int n_in, void* d_out, int out_size, void* d_ws, size_t ws_size, hipStream_t stream) {
  (void)in_sizes; (void)n_in; (void)out_size; (void)ws_size;
  (void)hipFuncSetAttribute((const void*)fused_decode, hipFuncAttributeMaxDynamicSharedMemorySize, LDS_BYTES);
  hipLaunchKernelGGL(fused_decode, dim3(NWG), dim3(NTHR), LDS_BYTES, stream,
                     (const u16*)d_in[0], (const u16*)d_in[1], (const u16*)d_in[2], (const u16*)d_in[3], (const u16*)d_in[4],
                     (const u16*)d_in[6], (const u16*)d_in[7], (const u16*)d_in[8], (const u16*)d_in[9], (u16*)d_out, (unsigned char*)d_ws);
}
```
